```python
import torch

def _softmax(x, axis=-1):
    return torch.softmax(x, dim=axis)
def _gelu(x):
    return torch.nn.functional.gelu(x, approximate="tanh")
def _silu(x):
    return torch.nn.functional.silu(x)
def _sigmoid(x):
    return torch.sigmoid(x)
def _layernorm(x, axis=-1, eps=1e-5):
    m = x.mean(dim=axis, keepdim=True)
    v = x.var(dim=axis, keepdim=True, unbiased=False)
    return (x - m) / torch.sqrt(v + eps)
def _scatter_add(tgt, idx, src, axis=0):
    return tgt.clone().index_add_(axis, idx, src)
def _segment_sum(src, seg, n):
    out = torch.zeros((n,) + src.shape[1:], dtype=src.dtype)
    return out.index_add_(0, seg, src)
def _pad(x, pw):
    flat = tuple(v for pair in reversed(pw) for v in pair)
    return torch.nn.functional.pad(x, flat)

kernel_name = 'standalone__gqa_softmax__near_ridge'

def setup_inputs():

    _w = (lambda a: (lambda t: t.to(torch.bfloat16) if t.is_floating_point() else t)(torch.as_tensor(a)))
    B, Hq, Hkv, Sq, Sk, D = 8, 32, 32, 192, 1024, 128
    return {
        "q": _w((lambda *s: torch.randn(*s, dtype=torch.bfloat16) * 0.8)(B, Hq, Sq, D)),
        "k": _w((lambda *s: torch.randn(*s, dtype=torch.bfloat16) * 0.8)(B, Hkv, Sk, D)),
        "v": _w((lambda *s: torch.randn(*s, dtype=torch.bfloat16) * 0.05)(B, Hkv, Sk, D)),
    }

def reference(q, k, v):
    B, Hq, Sq, D = q.shape
    Hkv = k.shape[1]
    rep = Hq // Hkv
    k = (lambda x, n, axis: torch.repeat_interleave(x, n, dim=axis))(k, rep, axis=1)
    v = (lambda x, n, axis: torch.repeat_interleave(x, n, dim=axis))(v, rep, axis=1)
    scale = D ** -0.5
    scores = torch.einsum("bhqd,bhkd->bhqk", q, k) * scale
    p = _softmax(scores, axis=-1)
    return torch.einsum("bhqk,bhkd->bhqd", p, v)
```

```cpp
#include <hip/hip_runtime.h>
#include <stdint.h>

typedef __bf16 bf16x8 __attribute__((ext_vector_type(8)));
typedef __bf16 bf16x4 __attribute__((ext_vector_type(4)));
typedef __bf16 bf16x2 __attribute__((ext_vector_type(2)));
typedef float f32x4 __attribute__((ext_vector_type(4)));
typedef float f32x2 __attribute__((ext_vector_type(2)));
typedef unsigned u32x4 __attribute__((ext_vector_type(4)));
typedef unsigned u32x2 __attribute__((ext_vector_type(2)));
#define LAS __attribute__((address_space(3)))
#define DI __device__ __forceinline__

#define NS 5
#define DEPTH 4
#define THR 8.0f
#define DMA_NT 1
#define DMAMOD " nt"

constexpr int SQ = 192, SK = 1024, HD = 128, NHEAD = 256;
constexpr int TK = 64, NT = SK / TK;
constexpr unsigned TILEB = TK * HD * 2;
constexpr unsigned KRING = 0, VRING = NS * TILEB, LDS_BYTES = 2 * NS * TILEB;
static_assert(DEPTH <= NS - 1 && DEPTH >= 1, "ring depth");
static_assert(LDS_BYTES <= 163840, "lds");

DI unsigned rfl(unsigned x) { return __builtin_amdgcn_readfirstlane(x); }
DI unsigned cvtpk(float lo, float hi) {
  f32x2 v = {lo, hi};
  bf16x2 b = __builtin_convertvector(v, bf16x2);
  return __builtin_bit_cast(unsigned, b);
}
DI float max3f(float a, float b, float c) { float r; asm("v_max3_f32 %0, %1, %2, %3" : "=v"(r) : "v"(a), "v"(b), "v"(c)); return r; }
DI float fma_s(float a, float b, float c) { float r; asm("v_fma_f32 %0, %1, %2, -%3" : "=v"(r) : "v"(a), "s"(b), "v"(c)); return r; }
DI float max2f(float a, float b) { float r; asm("v_max_f32_e32 %0, %1, %2" : "=v"(r) : "v"(a), "v"(b)); return r; }

DI void dma4(unsigned m0v, unsigned v0, unsigned v1, unsigned v2, unsigned v3, __amdgpu_buffer_rsrc_t srd, unsigned soff) {
  asm volatile("s_mov_b32 m0, %0\n\ts_nop 0\n\t"
               "buffer_load_dwordx4 %1, %5, %6 offen" DMAMOD " lds\n\t"
               "buffer_load_dwordx4 %2, %5, %6 offen offset:1024" DMAMOD " lds\n\t"
               "buffer_load_dwordx4 %3, %5, %6 offen offset:2048" DMAMOD " lds\n\t"
               "buffer_load_dwordx4 %4, %5, %6 offen offset:3072" DMAMOD " lds"
               :: "s"(m0v), "v"(v0), "v"(v1), "v"(v2), "v"(v3), "s"(srd), "s"(soff) : "memory");
}

template <int N> DI void wait_vm_barrier() {
  asm volatile("s_waitcnt vmcnt(%0) lgkmcnt(0)\n\ts_barrier" :: "i"(N) : "memory");
}

struct Ctx {
  unsigned kaddr[4], vaddr[4][2];
  unsigned vk[4], vv[2];
  unsigned m0k, m0v, sofw;
  __amdgpu_buffer_rsrc_t srdK, srdV;
  bf16x8 q[3][4];
  f32x4 o[3][8], l[3];
  float mc[3];
};

DI void issue_k(Ctx& c, int t, unsigned slot) { dma4(c.m0k + slot * TILEB, c.vk[0], c.vk[1], c.vk[2], c.vk[3], c.srdK, c.sofw + (unsigned)t * TILEB); }
DI void issue_v(Ctx& c, int t, unsigned slot) { dma4(c.m0v + slot * TILEB, c.vv[0], c.vv[1], c.vv[0], c.vv[1], c.srdV, c.sofw + (unsigned)t * TILEB); }

DI void qk(Ctx& c, f32x4 (&s)[4][3], unsigned kslot) {
#pragma unroll
  for (int kb = 0; kb < 4; ++kb) {
#pragma unroll
    for (int ks = 0; ks < 4; ++ks) {
      bf16x8 kf = *(const LAS bf16x8*)(c.kaddr[ks] + kslot + 4096u * kb);
#pragma unroll
      for (int qb = 0; qb < 3; ++qb) {
        if (ks == 0) s[kb][qb] = __builtin_amdgcn_mfma_f32_16x16x32_bf16(kf, c.q[qb][ks], (f32x4){0.f, 0.f, 0.f, 0.f}, 0, 0, 0);
        else         s[kb][qb] = __builtin_amdgcn_mfma_f32_16x16x32_bf16(kf, c.q[qb][ks], s[kb][qb], 0, 0, 0);
      }
    }
  }
}

DI void pv(Ctx& c, const bf16x8 (&p)[3][2], unsigned vslot) {
  const bf16x8 ones = __builtin_bit_cast(bf16x8, (u32x4){0x3f803f80u, 0x3f803f80u, 0x3f803f80u, 0x3f803f80u});
#pragma unroll
  for (int ks = 0; ks < 2; ++ks) {
#pragma unroll
    for (int qb = 0; qb < 3; ++qb) c.l[qb] = __builtin_amdgcn_mfma_f32_16x16x32_bf16(ones, p[qb][ks], c.l[qb], 0, 0, 0);
#pragma unroll
    for (int db = 0; db < 8; ++db) {
      bf16x4 lo = __builtin_amdgcn_ds_read_tr16_b64_v4bf16((LAS bf16x4*)(c.vaddr[db >> 1][db & 1] + vslot + 8192u * ks));
      bf16x4 hi = __builtin_amdgcn_ds_read_tr16_b64_v4bf16((LAS bf16x4*)(c.vaddr[db >> 1][db & 1] + vslot + 8192u * ks + 4096u));
      bf16x8 vf = __builtin_shufflevector(lo, hi, 0, 1, 2, 3, 4, 5, 6, 7);
#pragma unroll
      for (int qb = 0; qb < 3; ++qb) c.o[qb][db] = __builtin_amdgcn_mfma_f32_16x16x32_bf16(vf, p[qb][ks], c.o[qb][db], 0, 0, 0);
    }
  }
}

DI float rowmax16(const f32x4 (&s)[4][3], int qb) {
  float a = max3f(s[0][qb][0], s[0][qb][1], s[0][qb][2]);
  float b = max3f(s[0][qb][3], s[1][qb][0], s[1][qb][1]);
  a = max3f(a, s[1][qb][2], s[1][qb][3]);
  b = max3f(b, s[2][qb][0], s[2][qb][1]);
  a = max3f(a, s[2][qb][2], s[2][qb][3]);
  b = max3f(b, s[3][qb][0], s[3][qb][1]);
  a = max3f(a, s[3][qb][2], s[3][qb][3]);
  return max2f(a, b);
}
DI float xlane_max(float x) {
  auto r = __builtin_amdgcn_permlane16_swap(__float_as_uint(x), __float_as_uint(x), false, false);
  x = max2f(__uint_as_float(r[0]), __uint_as_float(r[1]));
  auto r2 = __builtin_amdgcn_permlane32_swap(__float_as_uint(x), __float_as_uint(x), false, false);
  return max2f(__uint_as_float(r2[0]), __uint_as_float(r2[1]));
}

DI void exp_pack(f32x4 (&s)[4][3], bf16x8 (&p)[3][2]) {
#pragma unroll
  for (int ks = 0; ks < 2; ++ks)
#pragma unroll
    for (int qb = 0; qb < 3; ++qb) {
      float e[8];
#pragma unroll
      for (int j = 0; j < 8; ++j) e[j] = __builtin_amdgcn_exp2f(s[2 * ks + (j >> 2)][qb][j & 3]);
      u32x4 w = {cvtpk(e[0], e[1]), cvtpk(e[2], e[3]), cvtpk(e[4], e[5]), cvtpk(e[6], e[7])};
      p[qb][ks] = __builtin_bit_cast(bf16x8, w);
    }
}

constexpr float CSC = 0.12751743082f;

template <int VM>
DI void step(Ctx& c, int t, unsigned& ks_, unsigned& vs_, const bf16x8 (&pp)[3][2], bf16x8 (&pn)[3][2], bool more_k, bool more_v) {
  wait_vm_barrier<VM>();
  {
    unsigned kn = ks_ + DEPTH; kn = kn >= NS ? kn - NS : kn;
    unsigned vn = vs_ + DEPTH; vn = vn >= NS ? vn - NS : vn;
    if (more_k) issue_k(c, t + DEPTH, kn);
    if (more_v) issue_v(c, t + DEPTH - 1, vn);
  }
  f32x4 s[4][3];
  qk(c, s, ks_ * TILEB);
  float mx[3];
#pragma unroll
  for (int qb = 0; qb < 3; ++qb) {
#pragma unroll
    for (int kb = 0; kb < 4; ++kb)
#pragma unroll
      for (int r = 0; r < 4; ++r) s[kb][qb][r] = fma_s(s[kb][qb][r], CSC, c.mc[qb]);
    mx[qb] = rowmax16(s, qb);
  }
  const float mall = max3f(mx[0], mx[1], mx[2]);
  float f[3];
  const bool rare = __builtin_amdgcn_ballot_w64(mall > THR) != 0ull;
  if (__builtin_expect(rare, 0)) {
#pragma unroll
    for (int qb = 0; qb < 3; ++qb) {
      const float dl = max2f(xlane_max(mx[qb]), 0.f);
      c.mc[qb] += dl;
      f[qb] = __builtin_amdgcn_exp2f(-dl);
#pragma unroll
      for (int kb = 0; kb < 4; ++kb) s[kb][qb] -= dl;
    }
  }
  pv(c, pp, vs_ * TILEB);
  exp_pack(s, pn);
  if (__builtin_expect(rare, 0)) {
#pragma unroll
    for (int qb = 0; qb < 3; ++qb) {
      c.l[qb] *= f[qb];
#pragma unroll
      for (int db = 0; db < 8; ++db) c.o[qb][db] *= f[qb];
    }
  }
  ks_ = ks_ + 1 == NS ? 0 : ks_ + 1;
  vs_ = vs_ + 1 == NS ? 0 : vs_ + 1;
}

__global__ __launch_bounds__(256, 1) void attn_fwd(const uint16_t* __restrict__ Q, const uint16_t* __restrict__ K,
                                                   const uint16_t* __restrict__ V, uint16_t* __restrict__ O) {
  extern __shared__ __attribute__((aligned(16))) char lds[];
  const unsigned lane = threadIdx.x & 63u, wid = rfl(threadIdx.x >> 6);
  const unsigned head = blockIdx.x;
  const unsigned ldsb = (unsigned)(size_t)lds;
  const unsigned i16 = lane & 15u, g = lane >> 4;
  Ctx c;
  c.srdK = __builtin_amdgcn_make_buffer_rsrc((void*)(K + (size_t)head * SK * HD), (short)0, SK * HD * 2, 0x00020000);
  c.srdV = __builtin_amdgcn_make_buffer_rsrc((void*)(V + (size_t)head * SK * HD), (short)0, SK * HD * 2, 0x00020000);
#pragma unroll
  for (int j = 0; j < 4; ++j) c.vk[j] = 256u * g + 16u * (i16 ^ (4u * j + g));
#pragma unroll
  for (int j = 0; j < 2; ++j) { const unsigned r7 = 4u * j + g, f = (r7 & 1u) | ((r7 & 6u) << 1); c.vv[j] = 256u * g + 16u * (i16 ^ f); }
  c.m0k = ldsb + KRING + 4096u * wid; c.m0v = ldsb + VRING + 4096u * wid; c.sofw = 4096u * wid;

  issue_k(c, 0, 0);
  u32x4 qr[3][4];
  {
    const uint16_t* qp = Q + ((size_t)head * SQ + 48u * wid + i16) * HD + 8u * g;
#pragma unroll
    for (int qb = 0; qb < 3; ++qb)
#pragma unroll
      for (int ks = 0; ks < 4; ++ks)
        asm volatile("global_load_dwordx4 %0, %1, off offset:%2" : "=v"(qr[qb][ks]) : "v"(qp + qb * 16 * HD), "i"(64 * ks) : "memory");
  }
#pragma unroll
  for (int t = 1; t < DEPTH; ++t) { issue_k(c, t, t); issue_v(c, t - 1, t - 1); }

#pragma unroll
  for (int ks = 0; ks < 4; ++ks) c.kaddr[ks] = ldsb + KRING + 256u * i16 + 16u * ((4u * ks + g) ^ i16);
  {
    const unsigned q4 = (lane >> 2) & 3u, p = lane & 3u, r7 = 4u * (g & 1u) + q4, f = (r7 & 1u) | ((r7 & 6u) << 1);
#pragma unroll
    for (int s = 0; s < 4; ++s)
#pragma unroll
      for (int b = 0; b < 2; ++b) c.vaddr[s][b] = ldsb + VRING + 256u * (4u * g + q4) + 16u * ((4u * s + p) ^ f) + 8u * (b ^ (p & 1u));
  }
#pragma unroll
  for (int qb = 0; qb < 3; ++qb) {
    c.l[qb] = (f32x4){0.f, 0.f, 0.f, 0.f};
#pragma unroll
    for (int db = 0; db < 8; ++db) c.o[qb][db] = (f32x4){0.f, 0.f, 0.f, 0.f};
  }
  asm volatile("s_waitcnt vmcnt(%12)\n\ts_barrier"
               : "+v"(qr[0][0]), "+v"(qr[0][1]), "+v"(qr[0][2]), "+v"(qr[0][3]), "+v"(qr[1][0]), "+v"(qr[1][1]), "+v"(qr[1][2]), "+v"(qr[1][3]),
                 "+v"(qr[2][0]), "+v"(qr[2][1]), "+v"(qr[2][2]), "+v"(qr[2][3])
               : "i"(8 * (DEPTH - 1)) : "memory");
#pragma unroll
  for (int qb = 0; qb < 3; ++qb)
#pragma unroll
    for (int ks = 0; ks < 4; ++ks) c.q[qb][ks] = __builtin_bit_cast(bf16x8, qr[qb][ks]);

  bf16x8 pa[3][2], pb[3][2];
  issue_k(c, DEPTH, DEPTH); issue_v(c, DEPTH - 1, DEPTH - 1);
  {
    f32x4 s[4][3];
    qk(c, s, 0);
#pragma unroll
    for (int qb = 0; qb < 3; ++qb) {
      const float m = xlane_max(rowmax16(s, qb));
      c.mc[qb] = m * CSC;
#pragma unroll
      for (int kb = 0; kb < 4; ++kb)
#pragma unroll
        for (int r = 0; r < 4; ++r) s[kb][qb][r] = fma_s(s[kb][qb][r], CSC, c.mc[qb]);
    }
    exp_pack(s, pa);
  }
  unsigned ks_ = 1, vs_ = 0;
  constexpr int TS = NT - DEPTH;
  int t = 1;
  for (; t + 1 < TS; t += 2) {
    step<8 * (DEPTH - 1)>(c, t, ks_, vs_, pa, pb, true, true);
    step<8 * (DEPTH - 1)>(c, t + 1, ks_, vs_, pb, pa, true, true);
  }
  static_assert(DEPTH == 4 || DEPTH == 2 || DEPTH == 3 || DEPTH == 1, "");
  if constexpr (DEPTH == 4) {
    step<24>(c, 11, ks_, vs_, pa, pb, true, true);
    step<24>(c, 12, ks_, vs_, pb, pa, false, true);
    step<20>(c, 13, ks_, vs_, pa, pb, false, false);
    step<12>(c, 14, ks_, vs_, pb, pa, false, false);
    step<4>(c, 15, ks_, vs_, pa, pb, false, false);
  } else if constexpr (DEPTH == 3) {
    step<16>(c, 13, ks_, vs_, pa, pb, false, true);
    step<12>(c, 14, ks_, vs_, pb, pa, false, false);
    step<4>(c, 15, ks_, vs_, pa, pb, false, false);
  } else if constexpr (DEPTH == 2) {
    step<8>(c, 13, ks_, vs_, pa, pb, true, true);
    step<8>(c, 14, ks_, vs_, pb, pa, false, true);
    step<4>(c, 15, ks_, vs_, pa, pb, false, false);
  } else {
    step<0>(c, 15, ks_, vs_, pa, pb, false, true);
  }
  wait_vm_barrier<0>();
  pv(c, pb, vs_ * TILEB);

  char* op = (char*)(O + ((size_t)head * SQ + 48u * wid + i16) * HD) + 16u * g;
  const bool odd = (g & 1u) != 0u;
#pragma unroll
  for (int qb = 0; qb < 3; ++qb) {
    const float il = __builtin_amdgcn_rcpf(c.l[qb][0]);
#pragma unroll
    for (int s = 0; s < 4; ++s) {
      const f32x4 a = c.o[qb][2 * s] * il, b = c.o[qb][2 * s + 1] * il;
      const unsigned a0 = cvtpk(a[0], a[1]), a1 = cvtpk(a[2], a[3]), b0 = cvtpk(b[0], b[1]), b1 = cvtpk(b[2], b[3]);
      u32x4 w = {odd ? b0 : a0, odd ? b1 : a1, odd ? a0 : b0, odd ? a1 : b1};
      *(u32x4*)(op + qb * 16 * HD * 2 + 64 * s) = w;
    }
  }
}

extern "C" void kernel_launch(void* const* d_in, const int* in_sizes, int n_in, void* d_out, int out_size, void* d_ws, size_t ws_size,
                              hipStream_t stream) {
  (void)in_sizes; (void)n_in; (void)out_size; (void)d_ws; (void)ws_size;
  (void)hipFuncSetAttribute((const void*)attn_fwd, hipFuncAttributeMaxDynamicSharedMemorySize, (int)LDS_BYTES);
  hipLaunchKernelGGL(attn_fwd, dim3(NHEAD), dim3(256), LDS_BYTES, stream, (const uint16_t*)d_in[0], (const uint16_t*)d_in[1],
                     (const uint16_t*)d_in[2], (uint16_t*)d_out);
}
```

```cpp
#include <hip/hip_runtime.h>
#include <stdint.h>

constexpr int SQ = 192, SK = 1024, HD = 128, NHEAD = 256;
constexpr unsigned LDS_BYTES = 148224;
typedef unsigned u32x4 __attribute__((ext_vector_type(4)));

__device__ __forceinline__ float true_row_max(const uint16_t* __restrict__ qrow, const uint16_t* __restrict__ Kh, unsigned g) {
  float m = -__builtin_inff();
#pragma unroll 1
  for (unsigned key = g; key < (unsigned)SK; key += 4) {
    float s = 0.f;
#pragma unroll 1
    for (int c = 0; c < HD / 8; ++c) {
      const u32x4 a = *(const u32x4*)(qrow + 8 * c), b = *(const u32x4*)(Kh + (size_t)key * HD + 8 * c);
#pragma unroll
      for (int e = 0; e < 4; ++e) {
        s = __builtin_fmaf(__uint_as_float(a[e] << 16), __uint_as_float(b[e] << 16), s);
        s = __builtin_fmaf(__uint_as_float(a[e] & 0xffff0000u), __uint_as_float(b[e] & 0xffff0000u), s);
      }
    }
    m = fmaxf(m, s);
  }
  auto r = __builtin_amdgcn_permlane16_swap(__float_as_uint(m), __float_as_uint(m), false, false);
  m = fmaxf(__uint_as_float(r[0]), __uint_as_float(r[1]));
  auto r2 = __builtin_amdgcn_permlane32_swap(__float_as_uint(m), __float_as_uint(m), false, false);
  m = fmaxf(__uint_as_float(r2[0]), __uint_as_float(r2[1]));
  return m * 0.12751743082f;
}

template <int PRE>
__device__ __forceinline__ void attn_pass(const void* ka, unsigned bid, unsigned tid, float& mc0, float& mc1, float& mc2) {
  unsigned flag;
  asm volatile(
      "s_memrealtime s[94:95]\n\t"
      "s_load_dwordx8 s[84:91], %[ka], 0\n\t"
      "v_lshrrev_b32_e32 v124, 6, %[tid]\n\t"
      "s_getpc_b64 s[48:49]\n\t"
      ".Lc0_%=:\n\t"
      "v_readfirstlane_b32 s39, v124\n\t"
      "v_lshlrev_b32_e32 v125, 6, %[tid]\n\t"
      "v_min_u32_e32 v125, .Lc1_%=-.Lc0_%=, v125\n\t"
      "global_load_dword v245, v125, s[48:49]\n\t"
      "v_bfe_u32 v124, %[tid], 4, 2\n\t"
      "v_and_b32_e32 v125, 15, %[tid]\n\t"
      "v_xor_b32_e32 v126, v124, v125\n\t"
      "v_lshlrev_b32_e32 v126, 4, v126\n\t"
      "v_lshl_or_b32 v10, v124, 8, v126\n\t"
      "v_xor_b32_e32 v226, 64, v10\n\t"
      "v_xor_b32_e32 v227, 128, v10\n\t"
      "v_xor_b32_e32 v228, 192, v10\n\t"
      "s_and_b32 s42, %[bid], 7\n\t"
      "s_lshl_b32 s42, s42, 5\n\t"
      "s_lshr_b32 s43, %[bid], 3\n\t"
      "s_add_u32 s42, s42, s43\n\t"
      "s_mul_i32 s43, s42, 0xc000\n\t"
      "s_lshl_b32 s45, s42, 18\n\t"
      "s_mul_i32 s36, s39, 0x3000\n\t"
      "s_lshl_b32 s37, s39, 12\n\t"
      "s_mov_b32 s30, 0xc000\n\t"
      "s_mov_b32 s31, 0x20000\n\t"
      "s_mov_b32 s22, 0x40000\n\t"
      "s_mov_b32 s23, 0x20000\n\t"
      "s_add_u32 m0, s36, 98560\n\t"
      "s_waitcnt lgkmcnt(0)\n\t"
      "s_add_u32 s28, s84, s43\n\t"
      "s_addc_u32 s29, s85, 0\n\t"
      "s_add_u32 s20, s86, s45\n\t"
      "s_addc_u32 s21, s87, 0\n\t"
      "s_add_u32 s24, s88, s45\n\t"
      "s_addc_u32 s25, s89, 0\n\t"
      "s_add_u32 s88, s90, s43\n\t"
      "s_addc_u32 s89, s91, 0\n\t"
      "v_mov_b32_e32 v128, s28\n\t"
      "v_mov_b32_e32 v129, s29\n\t"
      "v_writelane_b32 v128, s20, 1\n\t"
      "v_writelane_b32 v129, s21, 1\n\t"
      "v_writelane_b32 v128, s24, 2\n\t"
      "v_writelane_b32 v129, s25, 2\n\t"
      "v_writelane_b32 v128, s88, 3\n\t"
      "v_writelane_b32 v129, s89, 3\n\t"
      "global_load_dword v244, v[128:129], off\n\t"
      "buffer_load_dwordx4 v10, s[28:31], s36 offen nt lds\n\t"
      "buffer_load_dwordx4 v226, s[28:31], s36 offen offset:1024 nt lds\n\t"
      "buffer_load_dwordx4 v227, s[28:31], s36 offen offset:2048 nt lds\n\t"
      "buffer_load_dwordx4 v228, s[28:31], s36 offen offset:3072 nt lds\n\t"
      "s_add_u32 m0, s36, 102656\n\t"
      "s_add_u32 s44, s36, 4096\n\t"
      "buffer_load_dwordx4 v10, s[28:31], s44 offen nt lds\n\t"
      "buffer_load_dwordx4 v226, s[28:31], s44 offen offset:1024 nt lds\n\t"
      "buffer_load_dwordx4 v227, s[28:31], s44 offen offset:2048 nt lds\n\t"
      "buffer_load_dwordx4 v228, s[28:31], s44 offen offset:3072 nt lds\n\t"
      "s_add_u32 m0, s36, 106752\n\t"
      "s_add_u32 s44, s36, 8192\n\t"
      "buffer_load_dwordx4 v10, s[28:31], s44 offen nt lds\n\t"
      "buffer_load_dwordx4 v226, s[28:31], s44 offen offset:1024 nt lds\n\t"
      "buffer_load_dwordx4 v227, s[28:31], s44 offen offset:2048 nt lds\n\t"
      "buffer_load_dwordx4 v228, s[28:31], s44 offen offset:3072 nt lds\n\t"
      "s_lshl_b32 s44, s39, 11\n\t"
      "s_and_b32 s45, s39, 1\n\t"
      "s_lshl_b32 s45, s45, 7\n\t"
      "s_mov_b32 m0, s44\n\t"
      "v_xor_b32_e32 v130, s45, v10\n\t"
      "v_xor_b32_e32 v131, 64, v130\n\t"
      "buffer_load_dwordx4 v130, s[20:23], s44 offen nt lds\n\t"
      "buffer_load_dwordx4 v131, s[20:23], s44 offen offset:1024 nt lds\n\t"
      "s_add_u32 s44, s44, 0x2000\n\t"
      "s_mov_b32 m0, s44\n\t"
      "s_nop 0\n\t"
      "buffer_load_dwordx4 v130, s[20:23], s44 offen nt lds\n\t"
      "buffer_load_dwordx4 v131, s[20:23], s44 offen offset:1024 nt lds\n\t"
      "s_mov_b32 s40, s37\n\t"
      "v_lshl_or_b32 v8, v125, 8, v126\n\t"
      "v_lshlrev_b32_e32 v126, 2, v124\n\t"
      "v_xor_b32_e32 v126, v126, v125\n\t"
      "v_lshlrev_b32_e32 v126, 4, v126\n\t"
      "v_lshl_or_b32 v126, v124, 8, v126\n\t"
      "v_add_u32_e32 v11, 32, v126\n\t"
      "v_and_b32_e32 v126, 7, v125\n\t"
      "v_xor_b32_e32 v126, v126, v124\n\t"
      "v_lshlrev_b32_e32 v126, 4, v126\n\t"
      "v_lshl_or_b32 v126, v125, 7, v126\n\t"
      "v_add_u32_e32 v12, s36, v126\n\t"
      "v_and_b32_e32 v126, 63, %[tid]\n\t"
      "v_lshl_add_u32 v13, v126, 4, s36\n\t"
      "v_lshrrev_b32_e32 v126, 3, v126\n\t"
      "v_and_b32_e32 v127, 7, %[tid]\n\t"
      "v_xor_b32_e32 v127, v127, v126\n\t"
      "v_lshlrev_b32_e32 v127, 4, v127\n\t"
      "v_lshl_or_b32 v14, v126, 8, v127\n\t"
      "v_bfe_u32 v126, %[tid], 2, 2\n\t"
      "v_lshlrev_b32_e32 v125, 4, v125\n\t"
      "v_lshl_or_b32 v125, v126, 8, v125\n\t"
      "v_mul_u32_u24_e32 v124, 0x408, v124\n\t"
      "v_add_u32_e32 v124, 0x10000, v124\n\t"
      "v_add_u32_e32 v9, v124, v125\n\t"
      "s_sub_u32 s24, s24, 0x4020\n\t"
      "s_subb_u32 s25, s25, 0\n\t"
      "s_mov_b32 s26, 0x40020\n\t"
      "s_mov_b32 s27, 0x20000\n\t"
      "s_mov_b32 s90, 0xc000\n\t"
      "s_mov_b32 s91, 0x20000\n\t"
      "s_mul_i32 s38, s39, 0x1020\n\t"
      "s_add_u32 s38, s38, 0x10000\n\t"
      "s_add_u32 s43, s36, 98560\n\t"
      "s_sub_u32 s45, s37, s36\n\t"
      "s_add_u32 s45, s45, 0xc000\n\t"
      "v_subrev_u32_e32 v229, 8, v11\n\t"
      "v_subrev_u32_e32 v230, 16, v11\n\t"
      "v_subrev_u32_e32 v231, 24, v11\n\t"
      "s_add_u32 s40, s40, 0x4000\n\t"
      "s_mov_b32 s41, 0x3e0293ee\n\t"
      "s_mov_b32 s42, 0x3f803f80\n\t"
      "v_xor_b32_e32 v220, 64, v8\n\t"
      "v_xor_b32_e32 v221, 128, v8\n\t"
      "v_xor_b32_e32 v222, 192, v8\n\t"
      "v_xor_b32_e32 v223, 64, v125\n\t"
      "v_xor_b32_e32 v224, 128, v125\n\t"
      "v_xor_b32_e32 v225, 192, v125\n\t"
      "v_add_u32_e32 v223, v124, v223\n\t"
      "v_add_u32_e32 v224, v124, v224\n\t"
      "v_add_u32_e32 v225, v124, v225\n\t"
      "v_add_u32_e32 v240, s43, v8\n\t"
      "v_add_u32_e32 v241, s43, v220\n\t"
      "v_add_u32_e32 v242, s43, v221\n\t"
      "v_add_u32_e32 v243, s43, v222\n\t"
      "v_accvgpr_write_b32 a112, s42\n\t"
      "v_accvgpr_write_b32 a113, s42\n\t"
      "v_accvgpr_write_b32 a114, s42\n\t"
      "v_accvgpr_write_b32 a115, s42\n\t"
      "v_mov_b64_e32 v[112:113], 0\n\t"
      "v_mov_b64_e32 v[114:115], 0\n\t"
      "s_nop 1\n\t"
      "v_mfma_f32_32x32x16_bf16 v[16:31], v[112:115], v[112:115], 0\n\t"
      "v_mfma_f32_32x32x16_bf16 v[32:47], v[112:115], v[112:115], 0\n\t"
      "v_mfma_f32_32x32x16_bf16 v[48:63], v[112:115], v[112:115], 0\n\t"
      "v_mfma_f32_32x32x16_bf16 v[64:79], v[112:115], v[112:115], 0\n\t"
      "v_mfma_f32_32x32x16_bf16 v[80:95], v[112:115], v[112:115], 0\n\t"
      "v_mfma_f32_32x32x16_bf16 v[96:111], v[112:115], v[112:115], 0\n\t"
      "v_mfma_f32_16x16x32_bf16 a[116:119], v[112:115], v[112:115], 0\n\t"
      "v_mfma_f32_16x16x32_bf16 a[120:123], v[112:115], v[112:115], 0\n\t"
      "v_mfma_f32_16x16x32_bf16 a[124:127], v[112:115], v[112:115], 0\n\t"
      "v_mov_b32_e32 v234, s41\n\t"
      "v_mov_b32_e32 v235, s41\n\t"
      "v_mov_b32_e32 v238, 148160\n\t"
      "ds_write_b32 v238, v112\n\t"
      "v_add_u32_e32 v239, s45, v13\n\t"
      "s_mov_b32 s58, 28\n\t"
      "s_cmp_gt_i32 s58, 3\n\t"
      "s_cselect_b64 vcc, -1, 0\n\t"
      "s_getpc_b64 s[50:51]\n\t"
      ".Lpc_%=:\n\t"
      "s_add_u32 s50, s50, .Lgo_%=-.Lpc_%=\n\t"
      "s_addc_u32 s51, s51, 0\n\t"
      "s_waitcnt lgkmcnt(0)\n\t"
      ".Lsc_%=:\n\t"
      "ds_read_b128 v[172:175], v239 offset:0\n\t"
      "ds_read_b128 v[176:179], v239 offset:1024\n\t"
      "ds_read_b128 v[180:183], v239 offset:2048\n\t"
      "ds_read_b128 v[184:187], v239 offset:3072\n\t"
      "s_waitcnt lgkmcnt(3)\n\t"
      "v_lshlrev_b32_e32 v124, 16, v172\n\t"
      "v_and_b32_e32 v125, 0xffff0000, v172\n\t"
      "v_lshlrev_b32_e32 v126, 16, v173\n\t"
      "v_and_b32_e32 v127, 0xffff0000, v173\n\t"
      "v_lshlrev_b32_e32 v128, 16, v174\n\t"
      "v_and_b32_e32 v129, 0xffff0000, v174\n\t"
      "v_lshlrev_b32_e32 v130, 16, v175\n\t"
      "v_and_b32_e32 v131, 0xffff0000, v175\n\t"
      "v_pk_mul_f32 v[124:125], v[124:125], v[234:235]\n\t"
      "v_pk_mul_f32 v[126:127], v[126:127], v[234:235]\n\t"
      "v_pk_mul_f32 v[128:129], v[128:129], v[234:235]\n\t"
      "v_pk_mul_f32 v[130:131], v[130:131], v[234:235]\n\t"
      "v_cvt_pk_bf16_f32 v172, v124, v125\n\t"
      "v_cvt_pk_bf16_f32 v173, v126, v127\n\t"
      "v_cvt_pk_bf16_f32 v174, v128, v129\n\t"
      "v_cvt_pk_bf16_f32 v175, v130, v131\n\t"
      "ds_write_b128 v239, v[172:175] offset:0\n\t"
      "s_waitcnt lgkmcnt(3)\n\t"
      "v_lshlrev_b32_e32 v124, 16, v176\n\t"
      "v_and_b32_e32 v125, 0xffff0000, v176\n\t"
      "v_lshlrev_b32_e32 v126, 16, v177\n\t"
      "v_and_b32_e32 v127, 0xffff0000, v177\n\t"
      "v_lshlrev_b32_e32 v128, 16, v178\n\t"
      "v_and_b32_e32 v129, 0xffff0000, v178\n\t"
      "v_lshlrev_b32_e32 v130, 16, v179\n\t"
      "v_and_b32_e32 v131, 0xffff0000, v179\n\t"
      "v_pk_mul_f32 v[124:125], v[124:125], v[234:235]\n\t"
      "v_pk_mul_f32 v[126:127], v[126:127], v[234:235]\n\t"
      "v_pk_mul_f32 v[128:129], v[128:129], v[234:235]\n\t"
      "v_pk_mul_f32 v[130:131], v[130:131], v[234:235]\n\t"
      "v_cvt_pk_bf16_f32 v176, v124, v125\n\t"
      "v_cvt_pk_bf16_f32 v177, v126, v127\n\t"
      "v_cvt_pk_bf16_f32 v178, v128, v129\n\t"
      "v_cvt_pk_bf16_f32 v179, v130, v131\n\t"
      "ds_write_b128 v239, v[176:179] offset:1024\n\t"
      "s_waitcnt lgkmcnt(3)\n\t"
      "v_lshlrev_b32_e32 v124, 16, v180\n\t"
      "v_and_b32_e32 v125, 0xffff0000, v180\n\t"
      "v_lshlrev_b32_e32 v126, 16, v181\n\t"
      "v_and_b32_e32 v127, 0xffff0000, v181\n\t"
      "v_lshlrev_b32_e32 v128, 16, v182\n\t"
      "v_and_b32_e32 v129, 0xffff0000, v182\n\t"
      "v_lshlrev_b32_e32 v130, 16, v183\n\t"
      "v_and_b32_e32 v131, 0xffff0000, v183\n\t"
      "v_pk_mul_f32 v[124:125], v[124:125], v[234:235]\n\t"
      "v_pk_mul_f32 v[126:127], v[126:127], v[234:235]\n\t"
      "v_pk_mul_f32 v[128:129], v[128:129], v[234:235]\n\t"
      "v_pk_mul_f32 v[130:131], v[130:131], v[234:235]\n\t"
      "v_cvt_pk_bf16_f32 v180, v124, v125\n\t"
      "v_cvt_pk_bf16_f32 v181, v126, v127\n\t"
      "v_cvt_pk_bf16_f32 v182, v128, v129\n\t"
      "v_cvt_pk_bf16_f32 v183, v130, v131\n\t"
      "ds_write_b128 v239, v[180:183] offset:2048\n\t"
      "s_waitcnt lgkmcnt(3)\n\t"
      "v_lshlrev_b32_e32 v124, 16, v184\n\t"
      "v_and_b32_e32 v125, 0xffff0000, v184\n\t"
      "v_lshlrev_b32_e32 v126, 16, v185\n\t"
      "v_and_b32_e32 v127, 0xffff0000, v185\n\t"
      "v_lshlrev_b32_e32 v128, 16, v186\n\t"
      "v_and_b32_e32 v129, 0xffff0000, v186\n\t"
      "v_lshlrev_b32_e32 v130, 16, v187\n\t"
      "v_and_b32_e32 v131, 0xffff0000, v187\n\t"
      "v_pk_mul_f32 v[124:125], v[124:125], v[234:235]\n\t"
      "v_pk_mul_f32 v[126:127], v[126:127], v[234:235]\n\t"
      "v_pk_mul_f32 v[128:129], v[128:129], v[234:235]\n\t"
      "v_pk_mul_f32 v[130:131], v[130:131], v[234:235]\n\t"
      "v_cvt_pk_bf16_f32 v184, v124, v125\n\t"
      "v_cvt_pk_bf16_f32 v185, v126, v127\n\t"
      "v_cvt_pk_bf16_f32 v186, v128, v129\n\t"
      "v_cvt_pk_bf16_f32 v187, v130, v131\n\t"
      "ds_write_b128 v239, v[184:187] offset:3072\n\t"
      "v_add_u32_e32 v239, 4096, v239\n\t"
      "s_setpc_b64 s[50:51]\n\t"
      ".Lgo_%=:\n\t"
      "v_add_u32_e32 v239, 98560, v13\n\t"
      "s_waitcnt vmcnt(12)\n\t"
      "s_add_u32 m0, s37, 0x4000\n\t"
      "s_nop 0\n\t"
      "buffer_load_dwordx4 v10, s[20:23], s40 offen nt lds\n\t"
      "buffer_load_dwordx4 v226, s[20:23], s40 offen offset:1024 nt lds\n\t"
      "buffer_load_dwordx4 v227, s[20:23], s40 offen offset:2048 nt lds\n\t"
      "buffer_load_dwordx4 v228, s[20:23], s40 offen offset:3072 nt lds\n\t"
      "s_waitcnt lgkmcnt(0)\n\t"
      "s_call_b64 s[50:51], .Lsc_%=\n\t"
      "ds_read_b128 a[0:3], v240 offset:0\n\t"
      "ds_read_b128 a[4:7], v241 offset:0\n\t"
      "ds_read_b128 a[8:11], v242 offset:0\n\t"
      "ds_read_b128 a[12:15], v243 offset:0\n\t"
      "s_waitcnt vmcnt(12)\n\t"
      "s_add_u32 m0, s38, 0x0\n\t"
      "s_nop 0\n\t"
      "buffer_load_dwordx4 v11, s[24:27], s40 offen nt lds\n\t"
      "buffer_load_dwordx4 v229, s[24:27], s40 offen offset:1032 nt lds\n\t"
      "buffer_load_dwordx4 v230, s[24:27], s40 offen offset:2064 nt lds\n\t"
      "buffer_load_dwordx4 v231, s[24:27], s40 offen offset:3096 nt lds\n\t"
      "s_add_u32 s40, s40, 0x4000\n\t"
      "s_waitcnt lgkmcnt(0)\n\t"
      "s_call_b64 s[50:51], .Lsc_%=\n\t"
      "ds_read_b128 a[16:19], v240 offset:4096\n\t"
      "ds_read_b128 a[20:23], v241 offset:4096\n\t"
      "ds_read_b128 a[24:27], v242 offset:4096\n\t"
      "ds_read_b128 a[28:31], v243 offset:4096\n\t"
      "s_waitcnt vmcnt(12)\n\t"
      "s_add_u32 m0, s37, 0x8000\n\t"
      "s_nop 0\n\t"
      "buffer_load_dwordx4 v10, s[20:23], s40 offen nt lds\n\t"
      "buffer_load_dwordx4 v226, s[20:23], s40 offen offset:1024 nt lds\n\t"
      "buffer_load_dwordx4 v227, s[20:23], s40 offen offset:2048 nt lds\n\t"
      "buffer_load_dwordx4 v228, s[20:23], s40 offen offset:3072 nt lds\n\t"
      "s_waitcnt lgkmcnt(0)\n\t"
      "s_call_b64 s[50:51], .Lsc_%=\n\t"
      "ds_read_b128 a[32:35], v240 offset:8192\n\t"
      "ds_read_b128 a[36:39], v241 offset:8192\n\t"
      "ds_read_b128 a[40:43], v242 offset:8192\n\t"
      "ds_read_b128 a[44:47], v243 offset:8192\n\t"
      "s_waitcnt vmcnt(14)\n\t"
      "s_waitcnt lgkmcnt(0)\n\t"
      "s_barrier\n\t"
      "s_memrealtime s[50:51]\n\t"
      "s_memtime s[46:47]\n\t"
      "ds_read_b128 a[80:83], v8 offset:0\n\t"
      "ds_read_b128 a[84:87], v220 offset:0\n\t"
      "ds_read_b128 a[88:91], v221 offset:0\n\t"
      "ds_read_b128 a[92:95], v222 offset:0\n\t"
      "ds_read_b128 a[96:99], v8 offset:4096\n\t"
      "ds_read_b128 a[100:103], v220 offset:4096\n\t"
      "ds_read_b128 a[104:107], v221 offset:4096\n\t"
      "ds_read_b128 a[108:111], v222 offset:4096\n\t"
      "s_waitcnt lgkmcnt(7)\n\t"
      "v_mfma_f32_16x16x32_bf16 v[124:127], a[80:83], a[0:3], 0\n\t"
      "s_waitcnt lgkmcnt(6)\n\t"
      "v_mfma_f32_16x16x32_bf16 v[124:127], a[84:87], a[4:7], v[124:127]\n\t"
      "s_waitcnt lgkmcnt(5)\n\t"
      "v_mfma_f32_16x16x32_bf16 v[124:127], a[88:91], a[8:11], v[124:127]\n\t"
      "s_waitcnt lgkmcnt(4)\n\t"
      "v_mfma_f32_16x16x32_bf16 v[124:127], a[92:95], a[12:15], v[124:127]\n\t"
      "v_mfma_f32_16x16x32_bf16 v[128:131], a[80:83], a[16:19], 0\n\t"
      "v_mfma_f32_16x16x32_bf16 v[128:131], a[84:87], a[20:23], v[128:131]\n\t"
      "v_mfma_f32_16x16x32_bf16 v[128:131], a[88:91], a[24:27], v[128:131]\n\t"
      "v_mfma_f32_16x16x32_bf16 v[128:131], a[92:95], a[28:31], v[128:131]\n\t"
      "v_mfma_f32_16x16x32_bf16 v[132:135], a[80:83], a[32:35], 0\n\t"
      "v_mfma_f32_16x16x32_bf16 v[132:135], a[84:87], a[36:39], v[132:135]\n\t"
      "v_mfma_f32_16x16x32_bf16 v[132:135], a[88:91], a[40:43], v[132:135]\n\t"
      "v_mfma_f32_16x16x32_bf16 v[132:135], a[92:95], a[44:47], v[132:135]\n\t"
      "s_waitcnt lgkmcnt(3)\n\t"
      "v_mfma_f32_16x16x32_bf16 v[136:139], a[96:99], a[0:3], 0\n\t"
      "s_waitcnt lgkmcnt(2)\n\t"
      "v_mfma_f32_16x16x32_bf16 v[136:139], a[100:103], a[4:7], v[136:139]\n\t"
      "s_waitcnt vmcnt(12)\n\t"
      "s_barrier\n\t"
      "s_waitcnt lgkmcnt(1)\n\t"
      "v_mfma_f32_16x16x32_bf16 v[136:139], a[104:107], a[8:11], v[136:139]\n\t"
      "ds_read_b128 a[48:51], v8 offset:8192\n\t"
      "s_waitcnt lgkmcnt(1)\n\t"
      "v_mfma_f32_16x16x32_bf16 v[136:139], a[108:111], a[12:15], v[136:139]\n\t"
      "ds_read_b128 a[52:55], v220 offset:8192\n\t"
      "v_mfma_f32_16x16x32_bf16 v[140:143], a[96:99], a[16:19], 0\n\t"
      "ds_read_b128 a[56:59], v221 offset:8192\n\t"
      "v_mfma_f32_16x16x32_bf16 v[140:143], a[100:103], a[20:23], v[140:143]\n\t"
      "ds_read_b128 a[60:63], v222 offset:8192\n\t"
      "v_mfma_f32_16x16x32_bf16 v[140:143], a[104:107], a[24:27], v[140:143]\n\t"
      "ds_read_b128 a[64:67], v8 offset:12288\n\t"
      "v_mfma_f32_16x16x32_bf16 v[140:143], a[108:111], a[28:31], v[140:143]\n\t"
      "ds_read_b128 a[68:71], v220 offset:12288\n\t"
      "v_mfma_f32_16x16x32_bf16 v[144:147], a[96:99], a[32:35], 0\n\t"
      "ds_read_b128 a[72:75], v221 offset:12288\n\t"
      "v_mfma_f32_16x16x32_bf16 v[144:147], a[100:103], a[36:39], v[144:147]\n\t"
      "ds_read_b128 a[76:79], v222 offset:12288\n\t"
      "v_mfma_f32_16x16x32_bf16 v[144:147], a[104:107], a[40:43], v[144:147]\n\t"
      "v_mfma_f32_16x16x32_bf16 v[144:147], a[108:111], a[44:47], v[144:147]\n\t"
      "s_cmp_lg_u32 %[pre], 0\n\t"
      "s_cbranch_scc1 .Lpre_%=\n\t"
      "v_max3_f32 v233, v124, v125, v126\n\t"
      "v_max3_f32 v233, v233, v127, v136\n\t"
      "v_max3_f32 v233, v233, v137, v138\n\t"
      "v_max_f32_e32 v233, v233, v139\n\t"
      "v_max3_f32 v234, v128, v129, v130\n\t"
      "v_max3_f32 v234, v234, v131, v140\n\t"
      "v_max3_f32 v234, v234, v141, v142\n\t"
      "v_max_f32_e32 v234, v234, v143\n\t"
      "v_max3_f32 v235, v132, v133, v134\n\t"
      "v_max3_f32 v235, v235, v135, v144\n\t"
      "v_max3_f32 v235, v235, v145, v146\n\t"
      "v_max_f32_e32 v235, v235, v147\n\t"
      "v_mov_b32_e32 v232, v233\n\t"
      "v_mov_b32_e32 v236, v234\n\t"
      "v_mov_b32_e32 v237, v235\n\t"
      "v_permlane16_swap_b32_e32 v233, v232\n\t"
      "v_permlane16_swap_b32_e32 v234, v236\n\t"
      "v_permlane16_swap_b32_e32 v235, v237\n\t"
      "v_max_f32_e32 v233, v233, v232\n\t"
      "v_max_f32_e32 v234, v234, v236\n\t"
      "v_max_f32_e32 v235, v235, v237\n\t"
      "v_mov_b32_e32 v232, v233\n\t"
      "v_mov_b32_e32 v236, v234\n\t"
      "v_mov_b32_e32 v237, v235\n\t"
      "v_permlane32_swap_b32_e32 v233, v232\n\t"
      "v_permlane32_swap_b32_e32 v234, v236\n\t"
      "v_permlane32_swap_b32_e32 v235, v237\n\t"
      "v_max_f32_e32 v233, v233, v232\n\t"
      "v_max_f32_e32 v234, v234, v236\n\t"
      "v_max_f32_e32 v235, v235, v237\n\t"
      "v_mov_b32_e32 %[mc0], v233\n\t"
      "v_mov_b32_e32 %[mc1], v234\n\t"
      "v_mov_b32_e32 %[mc2], v235\n\t"
      ".Lpre_%=:\n\t"
      "v_sub_f32_e32 v112, 0, %[mc0]\n\t"
      "v_sub_f32_e32 v113, 0, %[mc0]\n\t"
      "v_sub_f32_e32 v114, 0, %[mc0]\n\t"
      "v_sub_f32_e32 v115, 0, %[mc0]\n\t"
      "v_sub_f32_e32 v116, 0, %[mc1]\n\t"
      "v_sub_f32_e32 v117, 0, %[mc1]\n\t"
      "v_sub_f32_e32 v118, 0, %[mc1]\n\t"
      "v_sub_f32_e32 v119, 0, %[mc1]\n\t"
      "v_sub_f32_e32 v120, 0, %[mc2]\n\t"
      "v_sub_f32_e32 v121, 0, %[mc2]\n\t"
      "v_sub_f32_e32 v122, 0, %[mc2]\n\t"
      "v_sub_f32_e32 v123, 0, %[mc2]\n\t"
      "s_waitcnt lgkmcnt(0)\n\t"
      "s_mov_b32 s53, 65\n\t"
      "s_mov_b32 s52, s50\n\t"
      "s_mov_b32 s47, s50\n\t"
      "s_mov_b32 s56, 54\n\t"
      "s_sub_u32 s48, s46, 0x100000\n\t"
      "s_sub_u32 s54, s50, s94\n\t"
      "s_cmp_lt_u32 s54, 290\n\t"
      "s_cselect_b32 s46, s48, s46\n\t"
      "s_cselect_b32 s53, 65, s53\n\t"
      "s_cselect_b32 s56, 54, s56\n\t"
      "v_add_f32_e32 v124, v124, v112\n\t"
      "v_mfma_f32_16x16x32_bf16 v[148:151], a[48:51], a[0:3], v[112:115]\n\t"
      "v_add_f32_e32 v125, v125, v112\n\t"
      "v_mfma_f32_16x16x32_bf16 v[148:151], a[52:55], a[4:7], v[148:151]\n\t"
      "v_exp_f32_e32 v124, v124\n\t"
      "v_add_f32_e32 v126, v126, v112\n\t"
      "v_mfma_f32_16x16x32_bf16 v[148:151], a[56:59], a[8:11], v[148:151]\n\t"
      "v_exp_f32_e32 v125, v125\n\t"
      "v_mfma_f32_16x16x32_bf16 v[148:151], a[60:63], a[12:15], v[148:151]\n\t"
      "v_add_f32_e32 v127, v127, v112\n\t"
      "v_cvt_pk_bf16_f32 v172, v124, v125\n\t"
      "v_exp_f32_e32 v126, v126\n\t"
      "v_mfma_f32_16x16x32_bf16 v[152:155], a[48:51], a[16:19], v[116:119]\n\t"
      "v_add_f32_e32 v136, v136, v112\n\t"
      "v_mfma_f32_16x16x32_bf16 v[152:155], a[52:55], a[20:23], v[152:155]\n\t"
      "v_exp_f32_e32 v127, v127\n\t"
      "s_nop 0\n\t"
      "v_cvt_pk_bf16_f32 v173, v126, v127\n\t"
      "v_mfma_f32_16x16x32_bf16 v[152:155], a[56:59], a[24:27], v[152:155]\n\t"
      "v_add_f32_e32 v137, v137, v112\n\t"
      "v_mfma_f32_16x16x32_bf16 v[152:155], a[60:63], a[28:31], v[152:155]\n\t"
      "v_exp_f32_e32 v136, v136\n\t"
      "v_add_f32_e32 v138, v138, v112\n\t"
      "v_mfma_f32_16x16x32_bf16 v[156:159], a[48:51], a[32:35], v[120:123]\n\t"
      "v_exp_f32_e32 v137, v137\n\t"
      "v_mfma_f32_16x16x32_bf16 v[156:159], a[52:55], a[36:39], v[156:159]\n\t"
      "v_add_f32_e32 v139, v139, v112\n\t"
      "v_cvt_pk_bf16_f32 v174, v136, v137\n\t"
      "v_exp_f32_e32 v138, v138\n\t"
      "v_mfma_f32_16x16x32_bf16 v[156:159], a[56:59], a[40:43], v[156:159]\n\t"
      "v_add_f32_e32 v128, v128, v116\n\t"
      "v_mfma_f32_16x16x32_bf16 v[156:159], a[60:63], a[44:47], v[156:159]\n\t"
      "v_exp_f32_e32 v139, v139\n\t"
      "v_add_f32_e32 v129, v129, v116\n\t"
      "v_cvt_pk_bf16_f32 v175, v138, v139\n\t"
      "v_mfma_f32_16x16x32_bf16 v[160:163], a[64:67], a[0:3], v[112:115]\n\t"
      "v_exp_f32_e32 v128, v128\n\t"
      "v_mfma_f32_16x16x32_bf16 v[160:163], a[68:71], a[4:7], v[160:163]\n\t"
      "v_add_f32_e32 v130, v130, v116\n\t"
      "v_exp_f32_e32 v129, v129\n\t"
      "v_mfma_f32_16x16x32_bf16 v[160:163], a[72:75], a[8:11], v[160:163]\n\t"
      "v_add_f32_e32 v131, v131, v116\n\t"
      "v_cvt_pk_bf16_f32 v176, v128, v129\n\t"
      "v_mfma_f32_16x16x32_bf16 v[160:163], a[76:79], a[12:15], v[160:163]\n\t"
      "v_exp_f32_e32 v130, v130\n\t"
      "v_add_f32_e32 v140, v140, v116\n\t"
      "v_mfma_f32_16x16x32_bf16 v[164:167], a[64:67], a[16:19], v[116:119]\n\t"
      "s_waitcnt vmcnt(8) lgkmcnt(0)\n\t"
      "v_exp_f32_e32 v131, v131\n\t"
      "v_mfma_f32_16x16x32_bf16 v[164:167], a[68:71], a[20:23], v[164:167]\n\t"
      "v_add_f32_e32 v141, v141, v116\n\t"
      "v_cvt_pk_bf16_f32 v177, v130, v131\n\t"
      "v_mfma_f32_16x16x32_bf16 v[164:167], a[72:75], a[24:27], v[164:167]\n\t"
      "s_barrier\n\t"
      "v_exp_f32_e32 v140, v140\n\t"
      "ds_read_b128 a[48:51], v8 offset:16384\n\t"
      "v_add_f32_e32 v142, v142, v116\n\t"
      "v_mfma_f32_16x16x32_bf16 v[164:167], a[76:79], a[28:31], v[164:167]\n\t"
      "s_add_u32 m0, s38, 0x4080\n\t"
      "v_exp_f32_e32 v141, v141\n\t"
      "v_mfma_f32_16x16x32_bf16 v[168:171], a[64:67], a[32:35], v[120:123]\n\t"
      "buffer_load_dwordx4 v11, s[24:27], s40 offen nt lds\n\t"
      "v_add_f32_e32 v143, v143, v116\n\t"
      "v_cvt_pk_bf16_f32 v178, v140, v141\n\t"
      "v_exp_f32_e32 v142, v142\n\t"
      "v_mfma_f32_16x16x32_bf16 v[168:171], a[68:71], a[36:39], v[168:171]\n\t"
      "buffer_load_dwordx4 v229, s[24:27], s40 offen offset:1032 nt lds\n\t"
      "v_add_f32_e32 v132, v132, v120\n\t"
      "v_mfma_f32_16x16x32_bf16 v[168:171], a[72:75], a[40:43], v[168:171]\n\t"
      "v_exp_f32_e32 v143, v143\n\t"
      "buffer_load_dwordx4 v230, s[24:27], s40 offen offset:2064 nt lds\n\t"
      "v_add_f32_e32 v133, v133, v120\n\t"
      "v_cvt_pk_bf16_f32 v179, v142, v143\n\t"
      "v_mfma_f32_16x16x32_bf16 v[168:171], a[76:79], a[44:47], v[168:171]\n\t"
      "s_waitcnt lgkmcnt(0)\n\t"
      "v_exp_f32_e32 v132, v132\n\t"
      "buffer_load_dwordx4 v231, s[24:27], s40 offen offset:3096 nt lds\n\t"
      "v_add_f32_e32 v134, v134, v120\n\t"
      "v_exp_f32_e32 v133, v133\n\t"
      "ds_read_b128 a[52:55], v220 offset:16384\n\t"
      "v_add_f32_e32 v135, v135, v120\n\t"
      "v_cvt_pk_bf16_f32 v180, v132, v133\n\t"
      "v_exp_f32_e32 v134, v134\n\t"
      "ds_read_b128 a[56:59], v221 offset:16384\n\t"
      "v_add_f32_e32 v144, v144, v120\n\t"
      "v_exp_f32_e32 v135, v135\n\t"
      "ds_read_b128 a[60:63], v222 offset:16384\n\t"
      "v_cvt_pk_bf16_f32 v181, v134, v135\n\t"
      "ds_read_b128 a[64:67], v8 offset:20480\n\t"
      "v_add_f32_e32 v145, v145, v120\n\t"
      "v_exp_f32_e32 v144, v144\n\t"
      "ds_read_b128 a[68:71], v220 offset:20480\n\t"
      "v_add_f32_e32 v146, v146, v120\n\t"
      "v_exp_f32_e32 v145, v145\n\t"
      "ds_read_b128 a[72:75], v221 offset:20480\n\t"
      "v_add_f32_e32 v147, v147, v120\n\t"
      "v_cvt_pk_bf16_f32 v182, v144, v145\n\t"
      "v_exp_f32_e32 v146, v146\n\t"
      "ds_read_b128 a[76:79], v222 offset:20480\n\t"
      "v_exp_f32_e32 v147, v147\n\t"
      "v_mfma_f32_16x16x32_bf16 v[124:127], a[48:51], a[0:3], v[112:115]\n\t"
      "v_cvt_pk_bf16_f32 v183, v146, v147\n\t"
      "s_waitcnt lgkmcnt(6)\n\t"
      "v_mfma_f32_16x16x32_bf16 v[124:127], a[52:55], a[4:7], v[124:127]\n\t"
      "s_add_u32 s40, s40, 0x4000\n\t"
      "v_exp_f32_e32 v148, v148\n\t"
      "s_memtime s[48:49]\n\t"
      "s_memrealtime s[42:43]\n\t"
      "s_waitcnt lgkmcnt(0)\n\t"
      "s_sub_u32 s48, s48, s46\n\t"
      "s_sub_u32 s42, s42, s47\n\t"
      "s_lshl_b32 s48, s48, 4\n\t"
      "s_mul_i32 s42, s42, 305\n\t"
      "s_cmp_lt_u32 s48, s42\n\t"
      "s_cselect_b64 vcc, 0, vcc\n\t"
      "s_cselect_b32 s53, 54, s53\n\t"
      "s_cselect_b32 s56, 54, s56\n\t"
      "s_waitcnt lgkmcnt(5)\n\t"
      "v_mfma_f32_16x16x32_bf16 v[124:127], a[56:59], a[8:11], v[124:127]\n\t"
      "s_cbranch_vccnz .Lrv1_%=\n\t"
      ".Lrr1_%=:\n\t"
      "v_exp_f32_e32 v149, v149\n\t"
      "s_waitcnt lgkmcnt(4)\n\t"
      "v_mfma_f32_16x16x32_bf16 v[124:127], a[60:63], a[12:15], v[124:127]\n\t"
      "v_cvt_pk_bf16_f32 v184, v148, v149\n\t"
      "v_exp_f32_e32 v150, v150\n\t"
      "v_mfma_f32_16x16x32_bf16 v[128:131], a[48:51], a[16:19], v[116:119]\n\t"
      "v_mfma_f32_16x16x32_bf16 v[128:131], a[52:55], a[20:23], v[128:131]\n\t"
      "v_exp_f32_e32 v151, v151\n\t"
      "s_nop 0\n\t"
      "v_cvt_pk_bf16_f32 v185, v150, v151\n\t"
      "v_mfma_f32_16x16x32_bf16 v[128:131], a[56:59], a[24:27], v[128:131]\n\t"
      "v_mfma_f32_16x16x32_bf16 v[128:131], a[60:63], a[28:31], v[128:131]\n\t"
      "v_exp_f32_e32 v160, v160\n\t"
      "v_mfma_f32_16x16x32_bf16 v[132:135], a[48:51], a[32:35], v[120:123]\n\t"
      "v_exp_f32_e32 v161, v161\n\t"
      "v_mfma_f32_16x16x32_bf16 v[132:135], a[52:55], a[36:39], v[132:135]\n\t"
      "v_cvt_pk_bf16_f32 v186, v160, v161\n\t"
      "v_exp_f32_e32 v162, v162\n\t"
      "v_mfma_f32_16x16x32_bf16 v[132:135], a[56:59], a[40:43], v[132:135]\n\t"
      "v_mfma_f32_16x16x32_bf16 v[132:135], a[60:63], a[44:47], v[132:135]\n\t"
      "s_waitcnt lgkmcnt(0)\n\t"
      "v_exp_f32_e32 v163, v163\n\t"
      "s_nop 0\n\t"
      "v_cvt_pk_bf16_f32 v187, v162, v163\n\t"
      "v_mfma_f32_16x16x32_bf16 v[136:139], a[64:67], a[0:3], v[112:115]\n\t"
      "v_exp_f32_e32 v152, v152\n\t"
      "v_mfma_f32_16x16x32_bf16 v[136:139], a[68:71], a[4:7], v[136:139]\n\t"
      "v_exp_f32_e32 v153, v153\n\t"
      "v_mfma_f32_16x16x32_bf16 v[136:139], a[72:75], a[8:11], v[136:139]\n\t"
      "v_cvt_pk_bf16_f32 v188, v152, v153\n\t"
      "v_mfma_f32_16x16x32_bf16 v[136:139], a[76:79], a[12:15], v[136:139]\n\t"
      "v_exp_f32_e32 v154, v154\n\t"
      "v_mfma_f32_16x16x32_bf16 v[140:143], a[64:67], a[16:19], v[116:119]\n\t"
      "s_waitcnt vmcnt(8) lgkmcnt(0)\n\t"
      "v_exp_f32_e32 v155, v155\n\t"
      "v_mfma_f32_16x16x32_bf16 v[140:143], a[68:71], a[20:23], v[140:143]\n\t"
      "v_cvt_pk_bf16_f32 v189, v154, v155\n\t"
      "v_mfma_f32_16x16x32_bf16 v[140:143], a[72:75], a[24:27], v[140:143]\n\t"
      "s_barrier\n\t"
      "v_exp_f32_e32 v164, v164\n\t"
      "ds_read_b128 a[48:51], v8 offset:24576\n\t"
      "v_mfma_f32_16x16x32_bf16 v[140:143], a[76:79], a[28:31], v[140:143]\n\t"
      "s_add_u32 m0, s37, 0xc000\n\t"
      "v_exp_f32_e32 v165, v165\n\t"
      "v_mfma_f32_16x16x32_bf16 v[144:147], a[64:67], a[32:35], v[120:123]\n\t"
      "buffer_load_dwordx4 v10, s[20:23], s40 offen nt lds\n\t"
      "v_cvt_pk_bf16_f32 v190, v164, v165\n\t"
      "v_exp_f32_e32 v166, v166\n\t"
      "v_mfma_f32_16x16x32_bf16 v[144:147], a[68:71], a[36:39], v[144:147]\n\t"
      "buffer_load_dwordx4 v226, s[20:23], s40 offen offset:1024 nt lds\n\t"
      "v_mfma_f32_16x16x32_bf16 v[144:147], a[72:75], a[40:43], v[144:147]\n\t"
      "v_exp_f32_e32 v167, v167\n\t"
      "buffer_load_dwordx4 v227, s[20:23], s40 offen offset:2048 nt lds\n\t"
      "v_cvt_pk_bf16_f32 v191, v166, v167\n\t"
      "v_mfma_f32_16x16x32_bf16 v[144:147], a[76:79], a[44:47], v[144:147]\n\t"
      "v_exp_f32_e32 v156, v156\n\t"
      "buffer_load_dwordx4 v228, s[20:23], s40 offen offset:3072 nt lds\n\t"
      "v_exp_f32_e32 v157, v157\n\t"
      "ds_read_b128 a[52:55], v220 offset:24576\n\t"
      "v_cvt_pk_bf16_f32 v192, v156, v157\n\t"
      "v_mfma_f32_16x16x32_bf16 a[116:119], a[112:115], v[172:175], a[116:119]\n\t"
      "v_exp_f32_e32 v158, v158\n\t"
      "ds_read_b128 a[56:59], v221 offset:24576\n\t"
      "v_mfma_f32_16x16x32_bf16 a[120:123], a[112:115], v[176:179], a[120:123]\n\t"
      "s_waitcnt lgkmcnt(1)\n\t"
      "v_exp_f32_e32 v159, v159\n\t"
      "v_mfma_f32_16x16x32_bf16 a[124:127], a[112:115], v[180:183], a[124:127]\n\t"
      "ds_read_b128 a[60:63], v222 offset:24576\n\t"
      "v_cvt_pk_bf16_f32 v193, v158, v159\n\t"
      "ds_read_b128 a[64:67], v8 offset:28672\n\t"
      "v_exp_f32_e32 v168, v168\n\t"
      "ds_read_b128 a[68:71], v220 offset:28672\n\t"
      "v_exp_f32_e32 v169, v169\n\t"
      "ds_read_b128 a[72:75], v221 offset:28672\n\t"
      "v_cvt_pk_bf16_f32 v194, v168, v169\n\t"
      "v_exp_f32_e32 v170, v170\n\t"
      "ds_read_b128 a[76:79], v222 offset:28672\n\t"
      "v_exp_f32_e32 v171, v171\n\t"
      "v_mfma_f32_16x16x32_bf16 v[148:151], a[48:51], a[0:3], v[112:115]\n\t"
      "ds_read_b64_tr_b16 a[80:81], v9 offset:0\n\t"
      "v_cvt_pk_bf16_f32 v195, v170, v171\n\t"
      "v_mfma_f32_16x16x32_bf16 v[148:151], a[52:55], a[4:7], v[148:151]\n\t"
      "s_waitcnt lgkmcnt(3)\n\t"
      "v_exp_f32_e32 v124, v124\n\t"
      "s_memtime s[48:49]\n\t"
      "s_memrealtime s[42:43]\n\t"
      "s_waitcnt lgkmcnt(0)\n\t"
      "s_sub_u32 s48, s48, s46\n\t"
      "s_sub_u32 s42, s42, s47\n\t"
      "s_lshl_b32 s48, s48, 4\n\t"
      "s_mul_i32 s42, s42, 311\n\t"
      "s_cmp_lt_u32 s48, s42\n\t"
      "s_cselect_b64 vcc, 0, vcc\n\t"
      "s_cselect_b32 s53, 54, s53\n\t"
      "s_cselect_b32 s56, 54, s56\n\t"
      "v_mfma_f32_16x16x32_bf16 v[148:151], a[56:59], a[8:11], v[148:151]\n\t"
      "ds_read_b64_tr_b16 a[82:83], v9 offset:4128\n\t"
      "v_mfma_f32_16x16x32_bf16 v[148:151], a[60:63], a[12:15], v[148:151]\n\t"
      "s_cbranch_vccnz .Lrv2_%=\n\t"
      ".Lrr2_%=:\n\t"
      "v_exp_f32_e32 v125, v125\n\t"
      "v_mfma_f32_16x16x32_bf16 v[152:155], a[48:51], a[16:19], v[116:119]\n\t"
      "ds_read_b64_tr_b16 a[84:85], v9 offset:8256\n\t"
      "v_cvt_pk_bf16_f32 v196, v124, v125\n\t"
      "v_mfma_f32_16x16x32_bf16 v[152:155], a[52:55], a[20:23], v[152:155]\n\t"
      "v_exp_f32_e32 v126, v126\n\t"
      "v_mfma_f32_16x16x32_bf16 v[152:155], a[56:59], a[24:27], v[152:155]\n\t"
      "ds_read_b64_tr_b16 a[86:87], v9 offset:12384\n\t"
      "v_mfma_f32_16x16x32_bf16 v[152:155], a[60:63], a[28:31], v[152:155]\n\t"
      "s_memrealtime s[50:51]\n\t"
      "v_exp_f32_e32 v127, v127\n\t"
      "v_mfma_f32_16x16x32_bf16 v[156:159], a[48:51], a[32:35], v[120:123]\n\t"
      "ds_read_b64_tr_b16 a[88:89], v9 offset:8\n\t"
      "v_cvt_pk_bf16_f32 v197, v126, v127\n\t"
      "v_mfma_f32_16x16x32_bf16 v[156:159], a[52:55], a[36:39], v[156:159]\n\t"
      "ds_read_b64_tr_b16 a[90:91], v9 offset:4136\n\t"
      "v_mfma_f32_16x16x32_bf16 v[156:159], a[56:59], a[40:43], v[156:159]\n\t"
      "v_exp_f32_e32 v136, v136\n\t"
      "v_mfma_f32_16x16x32_bf16 v[156:159], a[60:63], a[44:47], v[156:159]\n\t"
      "ds_read_b64_tr_b16 a[92:93], v9 offset:8264\n\t"
      "v_mfma_f32_16x16x32_bf16 v[160:163], a[64:67], a[0:3], v[112:115]\n\t"
      "s_waitcnt lgkmcnt(3)\n\t"
      "v_exp_f32_e32 v137, v137\n\t"
      "v_mfma_f32_16x16x32_bf16 v[160:163], a[68:71], a[4:7], v[160:163]\n\t"
      "ds_read_b64_tr_b16 a[94:95], v9 offset:12392\n\t"
      "v_cvt_pk_bf16_f32 v198, v136, v137\n\t"
      "v_mfma_f32_16x16x32_bf16 v[160:163], a[72:75], a[8:11], v[160:163]\n\t"
      "s_sub_u32 s54, s50, s52\n\t"
      "v_exp_f32_e32 v138, v138\n\t"
      "v_mfma_f32_16x16x32_bf16 v[160:163], a[76:79], a[12:15], v[160:163]\n\t"
      "ds_read_b64_tr_b16 a[96:97], v223 offset:0\n\t"
      "v_mfma_f32_16x16x32_bf16 v[164:167], a[64:67], a[16:19], v[116:119]\n\t"
      "s_cmp_lt_i32 s54, 0\n\t"
      "v_exp_f32_e32 v139, v139\n\t"
      "v_mfma_f32_16x16x32_bf16 v[164:167], a[68:71], a[20:23], v[164:167]\n\t"
      "ds_read_b64_tr_b16 a[98:99], v223 offset:4128\n\t"
      "v_cvt_pk_bf16_f32 v199, v138, v139\n\t"
      "v_mfma_f32_16x16x32_bf16 v[164:167], a[72:75], a[24:27], v[164:167]\n\t"
      "s_cbranch_scc1 .Lgv2_%=\n\t"
      ".Lgr2_%=:\n\t"
      "v_exp_f32_e32 v128, v128\n\t"
      "v_mfma_f32_16x16x32_bf16 v[164:167], a[76:79], a[28:31], v[164:167]\n\t"
      "ds_read_b64_tr_b16 a[100:101], v223 offset:8256\n\t"
      "v_mfma_f32_16x16x32_bf16 v[168:171], a[64:67], a[32:35], v[120:123]\n\t"
      "v_exp_f32_e32 v129, v129\n\t"
      "v_mfma_f32_16x16x32_bf16 v[168:171], a[68:71], a[36:39], v[168:171]\n\t"
      "ds_read_b64_tr_b16 a[102:103], v223 offset:12384\n\t"
      "v_cvt_pk_bf16_f32 v200, v128, v129\n\t"
      "v_mfma_f32_16x16x32_bf16 v[168:171], a[72:75], a[40:43], v[168:171]\n\t"
      "v_exp_f32_e32 v130, v130\n\t"
      "v_mfma_f32_16x16x32_bf16 v[168:171], a[76:79], a[44:47], v[168:171]\n\t"
      "ds_read_b64_tr_b16 a[104:105], v223 offset:8\n\t"
      "v_mfma_f32_16x16x32_bf16 a[116:119], a[112:115], v[184:187], a[116:119]\n\t"
      "s_memtime s[48:49]\n\t"
      "s_memrealtime s[42:43]\n\t"
      "s_waitcnt lgkmcnt(0)\n\t"
      "s_sub_u32 s48, s48, s46\n\t"
      "s_sub_u32 s42, s42, s47\n\t"
      "s_lshl_b32 s48, s48, 4\n\t"
      "s_mul_i32 s42, s42, 314\n\t"
      "s_cmp_lt_u32 s48, s42\n\t"
      "s_cselect_b64 vcc, 0, vcc\n\t"
      "s_cselect_b32 s53, 54, s53\n\t"
      "s_cselect_b32 s56, 54, s56\n\t"
      "s_branch .Lent_%=\n\t"
      ".Lgv2_%=:\n\t"
      "s_memrealtime s[50:51]\n\t"
      "s_sleep 1\n\t"
      "s_sub_u32 s54, s50, s52\n\t"
      "s_cmp_lt_i32 s54, 0\n\t"
      "s_cbranch_scc1 .Lgv2_%=\n\t"
      "s_branch .Lgr2_%=\n\t"
      ".Lgv3_%=:\n\t"
      "s_memrealtime s[50:51]\n\t"
      "s_sleep 1\n\t"
      "s_sub_u32 s54, s50, s52\n\t"
      "s_cmp_lt_i32 s54, 0\n\t"
      "s_cbranch_scc1 .Lgv3_%=\n\t"
      "s_branch .Lgr3_%=\n\t"
      ".Lgv4_%=:\n\t"
      "s_memrealtime s[50:51]\n\t"
      "s_sleep 1\n\t"
      "s_sub_u32 s54, s50, s52\n\t"
      "s_cmp_lt_i32 s54, 0\n\t"
      "s_cbranch_scc1 .Lgv4_%=\n\t"
      "s_branch .Lgr4_%=\n\t"
      ".Lgv5_%=:\n\t"
      "s_memrealtime s[50:51]\n\t"
      "s_sleep 1\n\t"
      "s_sub_u32 s54, s50, s52\n\t"
      "s_cmp_lt_i32 s54, 0\n\t"
      "s_cbranch_scc1 .Lgv5_%=\n\t"
      "s_branch .Lgr5_%=\n\t"
      ".Lgv6_%=:\n\t"
      "s_memrealtime s[50:51]\n\t"
      "s_sleep 1\n\t"
      "s_sub_u32 s54, s50, s52\n\t"
      "s_cmp_lt_i32 s54, 0\n\t"
      "s_cbranch_scc1 .Lgv6_%=\n\t"
      "s_branch .Lgr6_%=\n\t"
      ".Lgv7_%=:\n\t"
      "s_memrealtime s[50:51]\n\t"
      "s_sleep 1\n\t"
      "s_sub_u32 s54, s50, s52\n\t"
      "s_cmp_lt_i32 s54, 0\n\t"
      "s_cbranch_scc1 .Lgv7_%=\n\t"
      "s_branch .Lgr7_%=\n\t"
      ".Lgv8_%=:\n\t"
      "s_memrealtime s[50:51]\n\t"
      "s_sleep 1\n\t"
      "s_sub_u32 s54, s50, s52\n\t"
      "s_cmp_lt_i32 s54, 0\n\t"
      "s_cbranch_scc1 .Lgv8_%=\n\t"
      "s_branch .Lgr8_%=\n\t"
      ".Lgv9_%=:\n\t"
      "s_memrealtime s[50:51]\n\t"
      "s_sleep 1\n\t"
      "s_sub_u32 s54, s50, s52\n\t"
      "s_cmp_lt_i32 s54, 0\n\t"
      "s_cbranch_scc1 .Lgv9_%=\n\t"
      "s_branch .Lgr9_%=\n\t"
      ".Lgv10_%=:\n\t"
      "s_memrealtime s[50:51]\n\t"
      "s_sleep 1\n\t"
      "s_sub_u32 s54, s50, s52\n\t"
      "s_cmp_lt_i32 s54, 0\n\t"
      "s_cbranch_scc1 .Lgv10_%=\n\t"
      "s_branch .Lgr10_%=\n\t"
      ".Lrv1_%=:\n\t"
      "s_lshr_b32 s59, s58, 2\n\t"
      ".Lrl1_%=:\n\t"
      "s_sleep 1\n\t"
      "s_sub_u32 s59, s59, 1\n\t"
      "s_cmp_gt_i32 s59, 0\n\t"
      "s_cbranch_scc1 .Lrl1_%=\n\t"
      "s_sub_u32 s58, s58, 4\n\t"
      "s_cmp_gt_i32 s58, 3\n\t"
      "s_cselect_b64 vcc, -1, 0\n\t"
      "s_branch .Lrr1_%=\n\t"
      ".Lrv2_%=:\n\t"
      "s_lshr_b32 s59, s58, 2\n\t"
      ".Lrl2_%=:\n\t"
      "s_sleep 1\n\t"
      "s_sub_u32 s59, s59, 1\n\t"
      "s_cmp_gt_i32 s59, 0\n\t"
      "s_cbranch_scc1 .Lrl2_%=\n\t"
      "s_sub_u32 s58, s58, 4\n\t"
      "s_cmp_gt_i32 s58, 3\n\t"
      "s_cselect_b64 vcc, -1, 0\n\t"
      "s_branch .Lrr2_%=\n\t"
      ".Lrv3_%=:\n\t"
      "s_lshr_b32 s59, s58, 2\n\t"
      ".Lrl3_%=:\n\t"
      "s_sleep 1\n\t"
      "s_sub_u32 s59, s59, 1\n\t"
      "s_cmp_gt_i32 s59, 0\n\t"
      "s_cbranch_scc1 .Lrl3_%=\n\t"
      "s_sub_u32 s58, s58, 4\n\t"
      "s_cmp_gt_i32 s58, 3\n\t"
      "s_cselect_b64 vcc, -1, 0\n\t"
      "s_branch .Lrr3_%=\n\t"
      ".Lrv4_%=:\n\t"
      "s_lshr_b32 s59, s58, 2\n\t"
      ".Lrl4_%=:\n\t"
      "s_sleep 1\n\t"
      "s_sub_u32 s59, s59, 1\n\t"
      "s_cmp_gt_i32 s59, 0\n\t"
      "s_cbranch_scc1 .Lrl4_%=\n\t"
      "s_sub_u32 s58, s58, 4\n\t"
      "s_cmp_gt_i32 s58, 3\n\t"
      "s_cselect_b64 vcc, -1, 0\n\t"
      "s_branch .Lrr4_%=\n\t"
      ".Lrv5_%=:\n\t"
      "s_lshr_b32 s59, s58, 2\n\t"
      ".Lrl5_%=:\n\t"
      "s_sleep 1\n\t"
      "s_sub_u32 s59, s59, 1\n\t"
      "s_cmp_gt_i32 s59, 0\n\t"
      "s_cbranch_scc1 .Lrl5_%=\n\t"
      "s_sub_u32 s58, s58, 4\n\t"
      "s_cmp_gt_i32 s58, 3\n\t"
      "s_cselect_b64 vcc, -1, 0\n\t"
      "s_branch .Lrr5_%=\n\t"
      ".Lrv6_%=:\n\t"
      "s_lshr_b32 s59, s58, 2\n\t"
      ".Lrl6_%=:\n\t"
      "s_sleep 1\n\t"
      "s_sub_u32 s59, s59, 1\n\t"
      "s_cmp_gt_i32 s59, 0\n\t"
      "s_cbranch_scc1 .Lrl6_%=\n\t"
      "s_sub_u32 s58, s58, 4\n\t"
      "s_cmp_gt_i32 s58, 3\n\t"
      "s_cselect_b64 vcc, -1, 0\n\t"
      "s_branch .Lrr6_%=\n\t"
      ".Lrv7_%=:\n\t"
      "s_lshr_b32 s59, s58, 2\n\t"
      ".Lrl7_%=:\n\t"
      "s_sleep 1\n\t"
      "s_sub_u32 s59, s59, 1\n\t"
      "s_cmp_gt_i32 s59, 0\n\t"
      "s_cbranch_scc1 .Lrl7_%=\n\t"
      "s_sub_u32 s58, s58, 4\n\t"
      "s_cmp_gt_i32 s58, 3\n\t"
      "s_cselect_b64 vcc, -1, 0\n\t"
      "s_branch .Lrr7_%=\n\t"
      ".Lrv8_%=:\n\t"
      "s_lshr_b32 s59, s58, 2\n\t"
      ".Lrl8_%=:\n\t"
      "s_sleep 1\n\t"
      "s_sub_u32 s59, s59, 1\n\t"
      "s_cmp_gt_i32 s59, 0\n\t"
      "s_cbranch_scc1 .Lrl8_%=\n\t"
      "s_sub_u32 s58, s58, 4\n\t"
      "s_cmp_gt_i32 s58, 3\n\t"
      "s_cselect_b64 vcc, -1, 0\n\t"
      "s_branch .Lrr8_%=\n\t"
      ".Lrv9_%=:\n\t"
      "s_lshr_b32 s59, s58, 2\n\t"
      ".Lrl9_%=:\n\t"
      "s_sleep 1\n\t"
      "s_sub_u32 s59, s59, 1\n\t"
      "s_cmp_gt_i32 s59, 0\n\t"
      "s_cbranch_scc1 .Lrl9_%=\n\t"
      "s_sub_u32 s58, s58, 4\n\t"
      "s_cmp_gt_i32 s58, 3\n\t"
      "s_cselect_b64 vcc, -1, 0\n\t"
      "s_branch .Lrr9_%=\n\t"
      ".Lrv10_%=:\n\t"
      "s_lshr_b32 s59, s58, 2\n\t"
      ".Lrl10_%=:\n\t"
      "s_sleep 1\n\t"
      "s_sub_u32 s59, s59, 1\n\t"
      "s_cmp_gt_i32 s59, 0\n\t"
      "s_cbranch_scc1 .Lrl10_%=\n\t"
      "s_sub_u32 s58, s58, 4\n\t"
      "s_cmp_gt_i32 s58, 3\n\t"
      "s_cselect_b64 vcc, -1, 0\n\t"
      "s_branch .Lrr10_%=\n\t"
      ".Lvk_%=:\n\t"
      "s_add_u32 s20, s24, 32\n\t"
      "s_addc_u32 s21, s25, 0\n\t"
      "s_mov_b32 s22, 0x44000\n\t"
      "v_subrev_u32_e32 v10, 32, v11\n\t"
      "v_subrev_u32_e32 v226, 32, v11\n\t"
      "v_subrev_u32_e32 v227, 32, v11\n\t"
      "v_subrev_u32_e32 v228, 32, v11\n\t"
      "s_branch .Lvb_%=\n\t"
      ".Lloop_%=:\n\t"
      "s_waitcnt vmcnt(8) lgkmcnt(3)\n\t"
      "v_exp_f32_e32 v131, v131\n\t"
      "v_mfma_f32_16x16x32_bf16 a[120:123], a[112:115], v[188:191], a[120:123]\n\t"
      "ds_read_b64_tr_b16 a[106:107], v223 offset:37160\n\t"
      "ds_read_b64_tr_b16 a[108:109], v223 offset:41288\n\t"
      "v_mfma_f32_16x16x32_bf16 a[124:127], a[112:115], v[192:195], a[124:127]\n\t"
      "ds_read_b64_tr_b16 a[110:111], v223 offset:45416\n\t"
      "v_cvt_pk_bf16_f32 v201, v130, v131\n\t"
      "v_mfma_f32_16x16x32_bf16 v[16:19], a[80:83], v[172:175], v[16:19]\n\t"
      "s_barrier\n\t"
      "v_exp_f32_e32 v140, v140\n\t"
      "v_mfma_f32_16x16x32_bf16 v[16:19], a[84:87], v[184:187], v[16:19]\n\t"
      "ds_read_b128 a[48:51], v8 offset:0\n\t"
      "v_mfma_f32_16x16x32_bf16 v[48:51], a[80:83], v[176:179], v[48:51]\n\t"
      "s_add_u32 m0, s38, 0x0\n\t"
      "s_sub_u32 s54, s54, 8\n\t"
      "v_exp_f32_e32 v141, v141\n\t"
      "v_mfma_f32_16x16x32_bf16 v[48:51], a[84:87], v[188:191], v[48:51]\n\t"
      "buffer_load_dwordx4 v11, s[24:27], s40 offen nt lds\n\t"
      "v_cvt_pk_bf16_f32 v202, v140, v141\n\t"
      "v_mfma_f32_16x16x32_bf16 v[80:83], a[80:83], v[180:183], v[80:83]\n\t"
      "s_max_i32 s54, s54, 0\n\t"
      "v_exp_f32_e32 v142, v142\n\t"
      "v_mfma_f32_16x16x32_bf16 v[80:83], a[84:87], v[192:195], v[80:83]\n\t"
      "buffer_load_dwordx4 v229, s[24:27], s40 offen offset:1032 nt lds\n\t"
      "v_mfma_f32_16x16x32_bf16 v[20:23], a[88:91], v[172:175], v[20:23]\n\t"
      "s_add_u32 s52, s52, s54\n\t"
      "v_exp_f32_e32 v143, v143\n\t"
      "v_mfma_f32_16x16x32_bf16 v[20:23], a[92:95], v[184:187], v[20:23]\n\t"
      "buffer_load_dwordx4 v230, s[24:27], s40 offen offset:2064 nt lds\n\t"
      "v_cvt_pk_bf16_f32 v203, v142, v143\n\t"
      "v_mfma_f32_16x16x32_bf16 v[52:55], a[88:91], v[176:179], v[52:55]\n\t"
      "v_exp_f32_e32 v132, v132\n\t"
      "v_mfma_f32_16x16x32_bf16 v[52:55], a[92:95], v[188:191], v[52:55]\n\t"
      "buffer_load_dwordx4 v231, s[24:27], s40 offen offset:3096 nt lds\n\t"
      "v_mfma_f32_16x16x32_bf16 v[84:87], a[88:91], v[180:183], v[84:87]\n\t"
      "s_add_u32 s52, s52, s53\n\t"
      "v_exp_f32_e32 v133, v133\n\t"
      "v_mfma_f32_16x16x32_bf16 v[84:87], a[92:95], v[192:195], v[84:87]\n\t"
      "ds_read_b128 a[52:55], v220 offset:0\n\t"
      "v_cvt_pk_bf16_f32 v204, v132, v133\n\t"
      "v_mfma_f32_16x16x32_bf16 v[24:27], a[96:99], v[172:175], v[24:27]\n\t"
      "s_waitcnt lgkmcnt(1)\n\t"
      "v_exp_f32_e32 v134, v134\n\t"
      "v_mfma_f32_16x16x32_bf16 v[24:27], a[100:103], v[184:187], v[24:27]\n\t"
      "ds_read_b128 a[56:59], v221 offset:0\n\t"
      "v_mfma_f32_16x16x32_bf16 v[56:59], a[96:99], v[176:179], v[56:59]\n\t"
      "s_sub_u32 s53, s53, 1\n\t"
      "v_exp_f32_e32 v135, v135\n\t"
      "v_mfma_f32_16x16x32_bf16 v[56:59], a[100:103], v[188:191], v[56:59]\n\t"
      "ds_read_b128 a[60:63], v222 offset:0\n\t"
      "v_cvt_pk_bf16_f32 v205, v134, v135\n\t"
      "v_mfma_f32_16x16x32_bf16 v[88:91], a[96:99], v[180:183], v[88:91]\n\t"
      "ds_read_b128 a[64:67], v8 offset:4096\n\t"
      "v_mfma_f32_16x16x32_bf16 v[88:91], a[100:103], v[192:195], v[88:91]\n\t"
      "s_max_i32 s53, s53, s56\n\t"
      "v_exp_f32_e32 v144, v144\n\t"
      "v_mfma_f32_16x16x32_bf16 v[28:31], a[104:107], v[172:175], v[28:31]\n\t"
      "ds_read_b128 a[68:71], v220 offset:4096\n\t"
      "v_mfma_f32_16x16x32_bf16 v[28:31], a[108:111], v[184:187], v[28:31]\n\t"
      "v_exp_f32_e32 v145, v145\n\t"
      "v_mfma_f32_16x16x32_bf16 v[60:63], a[104:107], v[176:179], v[60:63]\n\t"
      "ds_read_b128 a[72:75], v221 offset:4096\n\t"
      "v_cvt_pk_bf16_f32 v206, v144, v145\n\t"
      "v_mfma_f32_16x16x32_bf16 v[60:63], a[108:111], v[188:191], v[60:63]\n\t"
      "v_exp_f32_e32 v146, v146\n\t"
      "v_mfma_f32_16x16x32_bf16 v[92:95], a[104:107], v[180:183], v[92:95]\n\t"
      "ds_read_b128 a[76:79], v222 offset:4096\n\t"
      "v_mfma_f32_16x16x32_bf16 v[92:95], a[108:111], v[192:195], v[92:95]\n\t"
      "s_waitcnt lgkmcnt(3)\n\t"
      "v_exp_f32_e32 v147, v147\n\t"
      "v_mfma_f32_16x16x32_bf16 v[124:127], a[48:51], a[0:3], v[112:115]\n\t"
      "ds_read_b64_tr_b16 a[80:81], v224 offset:33024\n\t"
      "v_cvt_pk_bf16_f32 v207, v146, v147\n\t"
      "v_mfma_f32_16x16x32_bf16 v[124:127], a[52:55], a[4:7], v[124:127]\n\t"
      "s_add_u32 s40, s40, 0x4000\n\t"
      "v_exp_f32_e32 v148, v148\n\t"
      "v_mfma_f32_16x16x32_bf16 v[124:127], a[56:59], a[8:11], v[124:127]\n\t"
      "ds_read_b64_tr_b16 a[82:83], v224 offset:37152\n\t"
      "v_mfma_f32_16x16x32_bf16 v[124:127], a[60:63], a[12:15], v[124:127]\n\t"
      "s_cbranch_vccnz .Lrv7_%=\n\t"
      ".Lrr7_%=:\n\t"
      "v_exp_f32_e32 v149, v149\n\t"
      "v_mfma_f32_16x16x32_bf16 v[128:131], a[48:51], a[16:19], v[116:119]\n\t"
      "ds_read_b64_tr_b16 a[84:85], v224 offset:41280\n\t"
      "v_cvt_pk_bf16_f32 v208, v148, v149\n\t"
      "v_mfma_f32_16x16x32_bf16 v[128:131], a[52:55], a[20:23], v[128:131]\n\t"
      "v_exp_f32_e32 v150, v150\n\t"
      "v_mfma_f32_16x16x32_bf16 v[128:131], a[56:59], a[24:27], v[128:131]\n\t"
      "ds_read_b64_tr_b16 a[86:87], v224 offset:45408\n\t"
      "v_mfma_f32_16x16x32_bf16 v[128:131], a[60:63], a[28:31], v[128:131]\n\t"
      "s_memrealtime s[50:51]\n\t"
      "v_exp_f32_e32 v151, v151\n\t"
      "v_mfma_f32_16x16x32_bf16 v[132:135], a[48:51], a[32:35], v[120:123]\n\t"
      "ds_read_b64_tr_b16 a[88:89], v224 offset:33032\n\t"
      "v_cvt_pk_bf16_f32 v209, v150, v151\n\t"
      "v_mfma_f32_16x16x32_bf16 v[132:135], a[52:55], a[36:39], v[132:135]\n\t"
      "ds_read_b64_tr_b16 a[90:91], v224 offset:37160\n\t"
      "v_mfma_f32_16x16x32_bf16 v[132:135], a[56:59], a[40:43], v[132:135]\n\t"
      "v_exp_f32_e32 v160, v160\n\t"
      "v_mfma_f32_16x16x32_bf16 v[132:135], a[60:63], a[44:47], v[132:135]\n\t"
      "ds_read_b64_tr_b16 a[92:93], v224 offset:41288\n\t"
      "v_mfma_f32_16x16x32_bf16 v[136:139], a[64:67], a[0:3], v[112:115]\n\t"
      "s_waitcnt lgkmcnt(3)\n\t"
      "v_exp_f32_e32 v161, v161\n\t"
      "v_mfma_f32_16x16x32_bf16 v[136:139], a[68:71], a[4:7], v[136:139]\n\t"
      "ds_read_b64_tr_b16 a[94:95], v224 offset:45416\n\t"
      "v_cvt_pk_bf16_f32 v210, v160, v161\n\t"
      "v_mfma_f32_16x16x32_bf16 v[136:139], a[72:75], a[8:11], v[136:139]\n\t"
      "s_sub_u32 s54, s50, s52\n\t"
      "v_exp_f32_e32 v162, v162\n\t"
      "v_mfma_f32_16x16x32_bf16 v[136:139], a[76:79], a[12:15], v[136:139]\n\t"
      "ds_read_b64_tr_b16 a[96:97], v225 offset:33024\n\t"
      "v_mfma_f32_16x16x32_bf16 v[140:143], a[64:67], a[16:19], v[116:119]\n\t"
      "s_cmp_lt_i32 s54, 0\n\t"
      "v_exp_f32_e32 v163, v163\n\t"
      "v_mfma_f32_16x16x32_bf16 v[140:143], a[68:71], a[20:23], v[140:143]\n\t"
      "ds_read_b64_tr_b16 a[98:99], v225 offset:37152\n\t"
      "v_cvt_pk_bf16_f32 v211, v162, v163\n\t"
      "v_mfma_f32_16x16x32_bf16 v[140:143], a[72:75], a[24:27], v[140:143]\n\t"
      "s_cbranch_scc1 .Lgv7_%=\n\t"
      ".Lgr7_%=:\n\t"
      "v_exp_f32_e32 v152, v152\n\t"
      "v_mfma_f32_16x16x32_bf16 v[140:143], a[76:79], a[28:31], v[140:143]\n\t"
      "ds_read_b64_tr_b16 a[100:101], v225 offset:41280\n\t"
      "v_mfma_f32_16x16x32_bf16 v[144:147], a[64:67], a[32:35], v[120:123]\n\t"
      "v_exp_f32_e32 v153, v153\n\t"
      "v_mfma_f32_16x16x32_bf16 v[144:147], a[68:71], a[36:39], v[144:147]\n\t"
      "ds_read_b64_tr_b16 a[102:103], v225 offset:45408\n\t"
      "v_cvt_pk_bf16_f32 v212, v152, v153\n\t"
      "v_mfma_f32_16x16x32_bf16 v[144:147], a[72:75], a[40:43], v[144:147]\n\t"
      "v_exp_f32_e32 v154, v154\n\t"
      "v_mfma_f32_16x16x32_bf16 v[144:147], a[76:79], a[44:47], v[144:147]\n\t"
      "ds_read_b64_tr_b16 a[104:105], v225 offset:33032\n\t"
      "v_mfma_f32_16x16x32_bf16 a[116:119], a[112:115], v[196:199], a[116:119]\n\t"
      "s_waitcnt vmcnt(8) lgkmcnt(3)\n\t"
      "v_exp_f32_e32 v155, v155\n\t"
      "v_mfma_f32_16x16x32_bf16 a[120:123], a[112:115], v[200:203], a[120:123]\n\t"
      "ds_read_b64_tr_b16 a[106:107], v225 offset:37160\n\t"
      "ds_read_b64_tr_b16 a[108:109], v225 offset:41288\n\t"
      "v_mfma_f32_16x16x32_bf16 a[124:127], a[112:115], v[204:207], a[124:127]\n\t"
      "ds_read_b64_tr_b16 a[110:111], v225 offset:45416\n\t"
      "v_cvt_pk_bf16_f32 v213, v154, v155\n\t"
      "v_mfma_f32_16x16x32_bf16 v[32:35], a[80:83], v[172:175], v[32:35]\n\t"
      "s_barrier\n\t"
      "v_exp_f32_e32 v164, v164\n\t"
      "v_mfma_f32_16x16x32_bf16 v[32:35], a[84:87], v[184:187], v[32:35]\n\t"
      "ds_read_b128 a[48:51], v8 offset:8192\n\t"
      "v_mfma_f32_16x16x32_bf16 v[64:67], a[80:83], v[176:179], v[64:67]\n\t"
      "s_add_u32 m0, s37, 0x8000\n\t"
      "s_sub_u32 s54, s54, 8\n\t"
      "v_exp_f32_e32 v165, v165\n\t"
      "v_mfma_f32_16x16x32_bf16 v[64:67], a[84:87], v[188:191], v[64:67]\n\t"
      "buffer_load_dwordx4 v10, s[20:23], s40 offen nt lds\n\t"
      "v_cvt_pk_bf16_f32 v214, v164, v165\n\t"
      "v_mfma_f32_16x16x32_bf16 v[96:99], a[80:83], v[180:183], v[96:99]\n\t"
      "s_max_i32 s54, s54, 0\n\t"
      "v_exp_f32_e32 v166, v166\n\t"
      "v_mfma_f32_16x16x32_bf16 v[96:99], a[84:87], v[192:195], v[96:99]\n\t"
      "buffer_load_dwordx4 v226, s[20:23], s40 offen offset:1024 nt lds\n\t"
      "v_mfma_f32_16x16x32_bf16 v[36:39], a[88:91], v[172:175], v[36:39]\n\t"
      "s_add_u32 s52, s52, s54\n\t"
      "v_exp_f32_e32 v167, v167\n\t"
      "v_mfma_f32_16x16x32_bf16 v[36:39], a[92:95], v[184:187], v[36:39]\n\t"
      "buffer_load_dwordx4 v227, s[20:23], s40 offen offset:2048 nt lds\n\t"
      "v_cvt_pk_bf16_f32 v215, v166, v167\n\t"
      "v_mfma_f32_16x16x32_bf16 v[68:71], a[88:91], v[176:179], v[68:71]\n\t"
      "v_exp_f32_e32 v156, v156\n\t"
      "v_mfma_f32_16x16x32_bf16 v[68:71], a[92:95], v[188:191], v[68:71]\n\t"
      "buffer_load_dwordx4 v228, s[20:23], s40 offen offset:3072 nt lds\n\t"
      "v_mfma_f32_16x16x32_bf16 v[100:103], a[88:91], v[180:183], v[100:103]\n\t"
      "s_add_u32 s52, s52, s53\n\t"
      "v_exp_f32_e32 v157, v157\n\t"
      "v_mfma_f32_16x16x32_bf16 v[100:103], a[92:95], v[192:195], v[100:103]\n\t"
      "ds_read_b128 a[52:55], v220 offset:8192\n\t"
      "v_cvt_pk_bf16_f32 v216, v156, v157\n\t"
      "v_mfma_f32_16x16x32_bf16 v[40:43], a[96:99], v[172:175], v[40:43]\n\t"
      "s_waitcnt lgkmcnt(1)\n\t"
      "v_exp_f32_e32 v158, v158\n\t"
      "v_mfma_f32_16x16x32_bf16 v[40:43], a[100:103], v[184:187], v[40:43]\n\t"
      "ds_read_b128 a[56:59], v221 offset:8192\n\t"
      "v_mfma_f32_16x16x32_bf16 v[72:75], a[96:99], v[176:179], v[72:75]\n\t"
      "s_sub_u32 s53, s53, 1\n\t"
      "v_exp_f32_e32 v159, v159\n\t"
      "v_mfma_f32_16x16x32_bf16 v[72:75], a[100:103], v[188:191], v[72:75]\n\t"
      "ds_read_b128 a[60:63], v222 offset:8192\n\t"
      "v_cvt_pk_bf16_f32 v217, v158, v159\n\t"
      "v_mfma_f32_16x16x32_bf16 v[104:107], a[96:99], v[180:183], v[104:107]\n\t"
      "ds_read_b128 a[64:67], v8 offset:12288\n\t"
      "v_mfma_f32_16x16x32_bf16 v[104:107], a[100:103], v[192:195], v[104:107]\n\t"
      "s_max_i32 s53, s53, s56\n\t"
      "v_exp_f32_e32 v168, v168\n\t"
      "v_mfma_f32_16x16x32_bf16 v[44:47], a[104:107], v[172:175], v[44:47]\n\t"
      "ds_read_b128 a[68:71], v220 offset:12288\n\t"
      "v_mfma_f32_16x16x32_bf16 v[44:47], a[108:111], v[184:187], v[44:47]\n\t"
      "v_exp_f32_e32 v169, v169\n\t"
      "v_mfma_f32_16x16x32_bf16 v[76:79], a[104:107], v[176:179], v[76:79]\n\t"
      "ds_read_b128 a[72:75], v221 offset:12288\n\t"
      "v_cvt_pk_bf16_f32 v218, v168, v169\n\t"
      "v_mfma_f32_16x16x32_bf16 v[76:79], a[108:111], v[188:191], v[76:79]\n\t"
      "v_exp_f32_e32 v170, v170\n\t"
      "v_mfma_f32_16x16x32_bf16 v[108:111], a[104:107], v[180:183], v[108:111]\n\t"
      "ds_read_b128 a[76:79], v222 offset:12288\n\t"
      "v_mfma_f32_16x16x32_bf16 v[108:111], a[108:111], v[192:195], v[108:111]\n\t"
      "s_waitcnt lgkmcnt(3)\n\t"
      "v_exp_f32_e32 v171, v171\n\t"
      "v_mfma_f32_16x16x32_bf16 v[148:151], a[48:51], a[0:3], v[112:115]\n\t"
      "ds_read_b64_tr_b16 a[80:81], v9 offset:49536\n\t"
      "v_cvt_pk_bf16_f32 v219, v170, v171\n\t"
      "v_mfma_f32_16x16x32_bf16 v[148:151], a[52:55], a[4:7], v[148:151]\n\t"
      "v_exp_f32_e32 v124, v124\n\t"
      "v_mfma_f32_16x16x32_bf16 v[148:151], a[56:59], a[8:11], v[148:151]\n\t"
      "ds_read_b64_tr_b16 a[82:83], v9 offset:53664\n\t"
      "v_mfma_f32_16x16x32_bf16 v[148:151], a[60:63], a[12:15], v[148:151]\n\t"
      "s_cbranch_vccnz .Lrv8_%=\n\t"
      ".Lrr8_%=:\n\t"
      "v_exp_f32_e32 v125, v125\n\t"
      "v_mfma_f32_16x16x32_bf16 v[152:155], a[48:51], a[16:19], v[116:119]\n\t"
      "ds_read_b64_tr_b16 a[84:85], v9 offset:57792\n\t"
      "v_cvt_pk_bf16_f32 v172, v124, v125\n\t"
      "v_mfma_f32_16x16x32_bf16 v[152:155], a[52:55], a[20:23], v[152:155]\n\t"
      "v_exp_f32_e32 v126, v126\n\t"
      "v_mfma_f32_16x16x32_bf16 v[152:155], a[56:59], a[24:27], v[152:155]\n\t"
      "ds_read_b64_tr_b16 a[86:87], v9 offset:61920\n\t"
      "v_mfma_f32_16x16x32_bf16 v[152:155], a[60:63], a[28:31], v[152:155]\n\t"
      "s_memrealtime s[50:51]\n\t"
      "v_exp_f32_e32 v127, v127\n\t"
      "v_mfma_f32_16x16x32_bf16 v[156:159], a[48:51], a[32:35], v[120:123]\n\t"
      "ds_read_b64_tr_b16 a[88:89], v9 offset:49544\n\t"
      "v_cvt_pk_bf16_f32 v173, v126, v127\n\t"
      "v_mfma_f32_16x16x32_bf16 v[156:159], a[52:55], a[36:39], v[156:159]\n\t"
      "ds_read_b64_tr_b16 a[90:91], v9 offset:53672\n\t"
      "v_mfma_f32_16x16x32_bf16 v[156:159], a[56:59], a[40:43], v[156:159]\n\t"
      "v_exp_f32_e32 v136, v136\n\t"
      "v_mfma_f32_16x16x32_bf16 v[156:159], a[60:63], a[44:47], v[156:159]\n\t"
      "ds_read_b64_tr_b16 a[92:93], v9 offset:57800\n\t"
      "v_mfma_f32_16x16x32_bf16 v[160:163], a[64:67], a[0:3], v[112:115]\n\t"
      "s_waitcnt lgkmcnt(3)\n\t"
      "v_exp_f32_e32 v137, v137\n\t"
      "v_mfma_f32_16x16x32_bf16 v[160:163], a[68:71], a[4:7], v[160:163]\n\t"
      "ds_read_b64_tr_b16 a[94:95], v9 offset:61928\n\t"
      "v_cvt_pk_bf16_f32 v174, v136, v137\n\t"
      "v_mfma_f32_16x16x32_bf16 v[160:163], a[72:75], a[8:11], v[160:163]\n\t"
      "s_sub_u32 s54, s50, s52\n\t"
      "v_exp_f32_e32 v138, v138\n\t"
      "v_mfma_f32_16x16x32_bf16 v[160:163], a[76:79], a[12:15], v[160:163]\n\t"
      "ds_read_b64_tr_b16 a[96:97], v223 offset:49536\n\t"
      "v_mfma_f32_16x16x32_bf16 v[164:167], a[64:67], a[16:19], v[116:119]\n\t"
      "s_cmp_lt_i32 s54, 0\n\t"
      "v_exp_f32_e32 v139, v139\n\t"
      "v_mfma_f32_16x16x32_bf16 v[164:167], a[68:71], a[20:23], v[164:167]\n\t"
      "ds_read_b64_tr_b16 a[98:99], v223 offset:53664\n\t"
      "v_cvt_pk_bf16_f32 v175, v138, v139\n\t"
      "v_mfma_f32_16x16x32_bf16 v[164:167], a[72:75], a[24:27], v[164:167]\n\t"
      "s_cbranch_scc1 .Lgv8_%=\n\t"
      ".Lgr8_%=:\n\t"
      "v_exp_f32_e32 v128, v128\n\t"
      "v_mfma_f32_16x16x32_bf16 v[164:167], a[76:79], a[28:31], v[164:167]\n\t"
      "ds_read_b64_tr_b16 a[100:101], v223 offset:57792\n\t"
      "v_mfma_f32_16x16x32_bf16 v[168:171], a[64:67], a[32:35], v[120:123]\n\t"
      "v_exp_f32_e32 v129, v129\n\t"
      "v_mfma_f32_16x16x32_bf16 v[168:171], a[68:71], a[36:39], v[168:171]\n\t"
      "ds_read_b64_tr_b16 a[102:103], v223 offset:61920\n\t"
      "v_cvt_pk_bf16_f32 v176, v128, v129\n\t"
      "v_mfma_f32_16x16x32_bf16 v[168:171], a[72:75], a[40:43], v[168:171]\n\t"
      "v_exp_f32_e32 v130, v130\n\t"
      "v_mfma_f32_16x16x32_bf16 v[168:171], a[76:79], a[44:47], v[168:171]\n\t"
      "ds_read_b64_tr_b16 a[104:105], v223 offset:49544\n\t"
      "v_mfma_f32_16x16x32_bf16 a[116:119], a[112:115], v[208:211], a[116:119]\n\t"
      "s_waitcnt vmcnt(8) lgkmcnt(3)\n\t"
      "v_exp_f32_e32 v131, v131\n\t"
      "v_mfma_f32_16x16x32_bf16 a[120:123], a[112:115], v[212:215], a[120:123]\n\t"
      "ds_read_b64_tr_b16 a[106:107], v223 offset:53672\n\t"
      "ds_read_b64_tr_b16 a[108:109], v223 offset:57800\n\t"
      "v_mfma_f32_16x16x32_bf16 a[124:127], a[112:115], v[216:219], a[124:127]\n\t"
      "ds_read_b64_tr_b16 a[110:111], v223 offset:61928\n\t"
      "v_cvt_pk_bf16_f32 v177, v130, v131\n\t"
      "v_mfma_f32_16x16x32_bf16 v[16:19], a[80:83], v[196:199], v[16:19]\n\t"
      "s_barrier\n\t"
      "v_exp_f32_e32 v140, v140\n\t"
      "v_mfma_f32_16x16x32_bf16 v[16:19], a[84:87], v[208:211], v[16:19]\n\t"
      "ds_read_b128 a[48:51], v8 offset:16384\n\t"
      "v_mfma_f32_16x16x32_bf16 v[48:51], a[80:83], v[200:203], v[48:51]\n\t"
      "s_add_u32 m0, s38, 0x4080\n\t"
      "s_sub_u32 s54, s54, 8\n\t"
      "v_exp_f32_e32 v141, v141\n\t"
      "v_mfma_f32_16x16x32_bf16 v[48:51], a[84:87], v[212:215], v[48:51]\n\t"
      "buffer_load_dwordx4 v11, s[24:27], s40 offen nt lds\n\t"
      "v_cvt_pk_bf16_f32 v178, v140, v141\n\t"
      "v_mfma_f32_16x16x32_bf16 v[80:83], a[80:83], v[204:207], v[80:83]\n\t"
      "s_max_i32 s54, s54, 0\n\t"
      "v_exp_f32_e32 v142, v142\n\t"
      "v_mfma_f32_16x16x32_bf16 v[80:83], a[84:87], v[216:219], v[80:83]\n\t"
      "buffer_load_dwordx4 v229, s[24:27], s40 offen offset:1032 nt lds\n\t"
      "v_mfma_f32_16x16x32_bf16 v[20:23], a[88:91], v[196:199], v[20:23]\n\t"
      "s_add_u32 s52, s52, s54\n\t"
      "v_exp_f32_e32 v143, v143\n\t"
      "v_mfma_f32_16x16x32_bf16 v[20:23], a[92:95], v[208:211], v[20:23]\n\t"
      "buffer_load_dwordx4 v230, s[24:27], s40 offen offset:2064 nt lds\n\t"
      "v_cvt_pk_bf16_f32 v179, v142, v143\n\t"
      "v_mfma_f32_16x16x32_bf16 v[52:55], a[88:91], v[200:203], v[52:55]\n\t"
      "v_exp_f32_e32 v132, v132\n\t"
      "v_mfma_f32_16x16x32_bf16 v[52:55], a[92:95], v[212:215], v[52:55]\n\t"
      "buffer_load_dwordx4 v231, s[24:27], s40 offen offset:3096 nt lds\n\t"
      "v_mfma_f32_16x16x32_bf16 v[84:87], a[88:91], v[204:207], v[84:87]\n\t"
      "s_add_u32 s52, s52, s53\n\t"
      "v_exp_f32_e32 v133, v133\n\t"
      "v_mfma_f32_16x16x32_bf16 v[84:87], a[92:95], v[216:219], v[84:87]\n\t"
      "ds_read_b128 a[52:55], v220 offset:16384\n\t"
      "v_cvt_pk_bf16_f32 v180, v132, v133\n\t"
      "v_mfma_f32_16x16x32_bf16 v[24:27], a[96:99], v[196:199], v[24:27]\n\t"
      "s_waitcnt lgkmcnt(1)\n\t"
      "v_exp_f32_e32 v134, v134\n\t"
      "v_mfma_f32_16x16x32_bf16 v[24:27], a[100:103], v[208:211], v[24:27]\n\t"
      "ds_read_b128 a[56:59], v221 offset:16384\n\t"
      "v_mfma_f32_16x16x32_bf16 v[56:59], a[96:99], v[200:203], v[56:59]\n\t"
      "s_sub_u32 s53, s53, 1\n\t"
      "v_exp_f32_e32 v135, v135\n\t"
      "v_mfma_f32_16x16x32_bf16 v[56:59], a[100:103], v[212:215], v[56:59]\n\t"
      "ds_read_b128 a[60:63], v222 offset:16384\n\t"
      "v_cvt_pk_bf16_f32 v181, v134, v135\n\t"
      "v_mfma_f32_16x16x32_bf16 v[88:91], a[96:99], v[204:207], v[88:91]\n\t"
      "ds_read_b128 a[64:67], v8 offset:20480\n\t"
      "v_mfma_f32_16x16x32_bf16 v[88:91], a[100:103], v[216:219], v[88:91]\n\t"
      "s_max_i32 s53, s53, s56\n\t"
      "v_exp_f32_e32 v144, v144\n\t"
      "v_mfma_f32_16x16x32_bf16 v[28:31], a[104:107], v[196:199], v[28:31]\n\t"
      "ds_read_b128 a[68:71], v220 offset:20480\n\t"
      "v_mfma_f32_16x16x32_bf16 v[28:31], a[108:111], v[208:211], v[28:31]\n\t"
      "v_exp_f32_e32 v145, v145\n\t"
      "v_mfma_f32_16x16x32_bf16 v[60:63], a[104:107], v[200:203], v[60:63]\n\t"
      "ds_read_b128 a[72:75], v221 offset:20480\n\t"
      "v_cvt_pk_bf16_f32 v182, v144, v145\n\t"
      "v_mfma_f32_16x16x32_bf16 v[60:63], a[108:111], v[212:215], v[60:63]\n\t"
      "v_exp_f32_e32 v146, v146\n\t"
      "v_mfma_f32_16x16x32_bf16 v[92:95], a[104:107], v[204:207], v[92:95]\n\t"
      "ds_read_b128 a[76:79], v222 offset:20480\n\t"
      "v_mfma_f32_16x16x32_bf16 v[92:95], a[108:111], v[216:219], v[92:95]\n\t"
      "s_waitcnt lgkmcnt(3)\n\t"
      "v_exp_f32_e32 v147, v147\n\t"
      "v_mfma_f32_16x16x32_bf16 v[124:127], a[48:51], a[0:3], v[112:115]\n\t"
      "ds_read_b64_tr_b16 a[80:81], v224 offset:49536\n\t"
      "v_cvt_pk_bf16_f32 v183, v146, v147\n\t"
      "v_mfma_f32_16x16x32_bf16 v[124:127], a[52:55], a[4:7], v[124:127]\n\t"
      "s_add_u32 s40, s40, 0x4000\n\t"
      "v_exp_f32_e32 v148, v148\n\t"
      "v_mfma_f32_16x16x32_bf16 v[124:127], a[56:59], a[8:11], v[124:127]\n\t"
      "ds_read_b64_tr_b16 a[82:83], v224 offset:53664\n\t"
      "v_mfma_f32_16x16x32_bf16 v[124:127], a[60:63], a[12:15], v[124:127]\n\t"
      "s_cbranch_vccnz .Lrv9_%=\n\t"
      ".Lrr9_%=:\n\t"
      "v_exp_f32_e32 v149, v149\n\t"
      "v_mfma_f32_16x16x32_bf16 v[128:131], a[48:51], a[16:19], v[116:119]\n\t"
      "ds_read_b64_tr_b16 a[84:85], v224 offset:57792\n\t"
      "v_cvt_pk_bf16_f32 v184, v148, v149\n\t"
      "v_mfma_f32_16x16x32_bf16 v[128:131], a[52:55], a[20:23], v[128:131]\n\t"
      "v_exp_f32_e32 v150, v150\n\t"
      "v_mfma_f32_16x16x32_bf16 v[128:131], a[56:59], a[24:27], v[128:131]\n\t"
      "ds_read_b64_tr_b16 a[86:87], v224 offset:61920\n\t"
      "v_mfma_f32_16x16x32_bf16 v[128:131], a[60:63], a[28:31], v[128:131]\n\t"
      "s_memrealtime s[50:51]\n\t"
      "v_exp_f32_e32 v151, v151\n\t"
      "v_mfma_f32_16x16x32_bf16 v[132:135], a[48:51], a[32:35], v[120:123]\n\t"
      "ds_read_b64_tr_b16 a[88:89], v224 offset:49544\n\t"
      "v_cvt_pk_bf16_f32 v185, v150, v151\n\t"
      "v_mfma_f32_16x16x32_bf16 v[132:135], a[52:55], a[36:39], v[132:135]\n\t"
      "ds_read_b64_tr_b16 a[90:91], v224 offset:53672\n\t"
      "v_mfma_f32_16x16x32_bf16 v[132:135], a[56:59], a[40:43], v[132:135]\n\t"
      "v_exp_f32_e32 v160, v160\n\t"
      "v_mfma_f32_16x16x32_bf16 v[132:135], a[60:63], a[44:47], v[132:135]\n\t"
      "ds_read_b64_tr_b16 a[92:93], v224 offset:57800\n\t"
      "v_mfma_f32_16x16x32_bf16 v[136:139], a[64:67], a[0:3], v[112:115]\n\t"
      "s_waitcnt lgkmcnt(3)\n\t"
      "v_exp_f32_e32 v161, v161\n\t"
      "v_mfma_f32_16x16x32_bf16 v[136:139], a[68:71], a[4:7], v[136:139]\n\t"
      "ds_read_b64_tr_b16 a[94:95], v224 offset:61928\n\t"
      "v_cvt_pk_bf16_f32 v186, v160, v161\n\t"
      "v_mfma_f32_16x16x32_bf16 v[136:139], a[72:75], a[8:11], v[136:139]\n\t"
      "s_sub_u32 s54, s50, s52\n\t"
      "v_exp_f32_e32 v162, v162\n\t"
      "v_mfma_f32_16x16x32_bf16 v[136:139], a[76:79], a[12:15], v[136:139]\n\t"
      "ds_read_b64_tr_b16 a[96:97], v225 offset:49536\n\t"
      "v_mfma_f32_16x16x32_bf16 v[140:143], a[64:67], a[16:19], v[116:119]\n\t"
      "s_cmp_lt_i32 s54, 0\n\t"
      "v_exp_f32_e32 v163, v163\n\t"
      "v_mfma_f32_16x16x32_bf16 v[140:143], a[68:71], a[20:23], v[140:143]\n\t"
      "ds_read_b64_tr_b16 a[98:99], v225 offset:53664\n\t"
      "v_cvt_pk_bf16_f32 v187, v162, v163\n\t"
      "v_mfma_f32_16x16x32_bf16 v[140:143], a[72:75], a[24:27], v[140:143]\n\t"
      "s_cbranch_scc1 .Lgv9_%=\n\t"
      ".Lgr9_%=:\n\t"
      "v_exp_f32_e32 v152, v152\n\t"
      "v_mfma_f32_16x16x32_bf16 v[140:143], a[76:79], a[28:31], v[140:143]\n\t"
      "ds_read_b64_tr_b16 a[100:101], v225 offset:57792\n\t"
      "v_mfma_f32_16x16x32_bf16 v[144:147], a[64:67], a[32:35], v[120:123]\n\t"
      "v_exp_f32_e32 v153, v153\n\t"
      "v_mfma_f32_16x16x32_bf16 v[144:147], a[68:71], a[36:39], v[144:147]\n\t"
      "ds_read_b64_tr_b16 a[102:103], v225 offset:61920\n\t"
      "v_cvt_pk_bf16_f32 v188, v152, v153\n\t"
      "v_mfma_f32_16x16x32_bf16 v[144:147], a[72:75], a[40:43], v[144:147]\n\t"
      "v_exp_f32_e32 v154, v154\n\t"
      "v_mfma_f32_16x16x32_bf16 v[144:147], a[76:79], a[44:47], v[144:147]\n\t"
      "ds_read_b64_tr_b16 a[104:105], v225 offset:49544\n\t"
      "v_mfma_f32_16x16x32_bf16 a[116:119], a[112:115], v[172:175], a[116:119]\n\t"
      "s_waitcnt vmcnt(8) lgkmcnt(3)\n\t"
      "v_exp_f32_e32 v155, v155\n\t"
      "v_mfma_f32_16x16x32_bf16 a[120:123], a[112:115], v[176:179], a[120:123]\n\t"
      "ds_read_b64_tr_b16 a[106:107], v225 offset:53672\n\t"
      "ds_read_b64_tr_b16 a[108:109], v225 offset:57800\n\t"
      "v_mfma_f32_16x16x32_bf16 a[124:127], a[112:115], v[180:183], a[124:127]\n\t"
      "ds_read_b64_tr_b16 a[110:111], v225 offset:61928\n\t"
      "v_cvt_pk_bf16_f32 v189, v154, v155\n\t"
      "v_mfma_f32_16x16x32_bf16 v[32:35], a[80:83], v[196:199], v[32:35]\n\t"
      "s_barrier\n\t"
      "v_exp_f32_e32 v164, v164\n\t"
      "v_mfma_f32_16x16x32_bf16 v[32:35], a[84:87], v[208:211], v[32:35]\n\t"
      "ds_read_b128 a[48:51], v8 offset:24576\n\t"
      "v_mfma_f32_16x16x32_bf16 v[64:67], a[80:83], v[200:203], v[64:67]\n\t"
      "s_add_u32 m0, s37, 0xc000\n\t"
      "s_sub_u32 s54, s54, 8\n\t"
      "v_exp_f32_e32 v165, v165\n\t"
      "v_mfma_f32_16x16x32_bf16 v[64:67], a[84:87], v[212:215], v[64:67]\n\t"
      "buffer_load_dwordx4 v10, s[20:23], s40 offen nt lds\n\t"
      "v_cvt_pk_bf16_f32 v190, v164, v165\n\t"
      "v_mfma_f32_16x16x32_bf16 v[96:99], a[80:83], v[204:207], v[96:99]\n\t"
      "s_max_i32 s54, s54, 0\n\t"
      "v_exp_f32_e32 v166, v166\n\t"
      "v_mfma_f32_16x16x32_bf16 v[96:99], a[84:87], v[216:219], v[96:99]\n\t"
      "buffer_load_dwordx4 v226, s[20:23], s40 offen offset:1024 nt lds\n\t"
      "v_mfma_f32_16x16x32_bf16 v[36:39], a[88:91], v[196:199], v[36:39]\n\t"
      "s_add_u32 s52, s52, s54\n\t"
      "v_exp_f32_e32 v167, v167\n\t"
      "v_mfma_f32_16x16x32_bf16 v[36:39], a[92:95], v[208:211], v[36:39]\n\t"
      "buffer_load_dwordx4 v227, s[20:23], s40 offen offset:2048 nt lds\n\t"
      "v_cvt_pk_bf16_f32 v191, v166, v167\n\t"
      "v_mfma_f32_16x16x32_bf16 v[68:71], a[88:91], v[200:203], v[68:71]\n\t"
      "v_exp_f32_e32 v156, v156\n\t"
      "v_mfma_f32_16x16x32_bf16 v[68:71], a[92:95], v[212:215], v[68:71]\n\t"
      "buffer_load_dwordx4 v228, s[20:23], s40 offen offset:3072 nt lds\n\t"
      "v_mfma_f32_16x16x32_bf16 v[100:103], a[88:91], v[204:207], v[100:103]\n\t"
      "s_add_u32 s52, s52, s53\n\t"
      "v_exp_f32_e32 v157, v157\n\t"
      "v_mfma_f32_16x16x32_bf16 v[100:103], a[92:95], v[216:219], v[100:103]\n\t"
      "ds_read_b128 a[52:55], v220 offset:24576\n\t"
      "v_cvt_pk_bf16_f32 v192, v156, v157\n\t"
      "v_mfma_f32_16x16x32_bf16 v[40:43], a[96:99], v[196:199], v[40:43]\n\t"
      "s_waitcnt lgkmcnt(1)\n\t"
      "v_exp_f32_e32 v158, v158\n\t"
      "v_mfma_f32_16x16x32_bf16 v[40:43], a[100:103], v[208:211], v[40:43]\n\t"
      "ds_read_b128 a[56:59], v221 offset:24576\n\t"
      "v_mfma_f32_16x16x32_bf16 v[72:75], a[96:99], v[200:203], v[72:75]\n\t"
      "s_sub_u32 s53, s53, 1\n\t"
      "v_exp_f32_e32 v159, v159\n\t"
      "v_mfma_f32_16x16x32_bf16 v[72:75], a[100:103], v[212:215], v[72:75]\n\t"
      "ds_read_b128 a[60:63], v222 offset:24576\n\t"
      "v_cvt_pk_bf16_f32 v193, v158, v159\n\t"
      "v_mfma_f32_16x16x32_bf16 v[104:107], a[96:99], v[204:207], v[104:107]\n\t"
      "ds_read_b128 a[64:67], v8 offset:28672\n\t"
      "v_mfma_f32_16x16x32_bf16 v[104:107], a[100:103], v[216:219], v[104:107]\n\t"
      "s_max_i32 s53, s53, s56\n\t"
      "v_exp_f32_e32 v168, v168\n\t"
      "v_mfma_f32_16x16x32_bf16 v[44:47], a[104:107], v[196:199], v[44:47]\n\t"
      "ds_read_b128 a[68:71], v220 offset:28672\n\t"
      "v_mfma_f32_16x16x32_bf16 v[44:47], a[108:111], v[208:211], v[44:47]\n\t"
      "v_exp_f32_e32 v169, v169\n\t"
      "v_mfma_f32_16x16x32_bf16 v[76:79], a[104:107], v[200:203], v[76:79]\n\t"
      "ds_read_b128 a[72:75], v221 offset:28672\n\t"
      "v_cvt_pk_bf16_f32 v194, v168, v169\n\t"
      "v_mfma_f32_16x16x32_bf16 v[76:79], a[108:111], v[212:215], v[76:79]\n\t"
      "v_exp_f32_e32 v170, v170\n\t"
      "v_mfma_f32_16x16x32_bf16 v[108:111], a[104:107], v[204:207], v[108:111]\n\t"
      "ds_read_b128 a[76:79], v222 offset:28672\n\t"
      "v_mfma_f32_16x16x32_bf16 v[108:111], a[108:111], v[216:219], v[108:111]\n\t"
      "s_waitcnt lgkmcnt(3)\n\t"
      "v_exp_f32_e32 v171, v171\n\t"
      "v_mfma_f32_16x16x32_bf16 v[148:151], a[48:51], a[0:3], v[112:115]\n\t"
      "ds_read_b64_tr_b16 a[80:81], v9 offset:0\n\t"
      "v_cvt_pk_bf16_f32 v195, v170, v171\n\t"
      "v_mfma_f32_16x16x32_bf16 v[148:151], a[52:55], a[4:7], v[148:151]\n\t"
      "v_exp_f32_e32 v124, v124\n\t"
      "v_mfma_f32_16x16x32_bf16 v[148:151], a[56:59], a[8:11], v[148:151]\n\t"
      "ds_read_b64_tr_b16 a[82:83], v9 offset:4128\n\t"
      "v_mfma_f32_16x16x32_bf16 v[148:151], a[60:63], a[12:15], v[148:151]\n\t"
      "s_cbranch_vccnz .Lrv10_%=\n\t"
      ".Lrr10_%=:\n\t"
      "v_exp_f32_e32 v125, v125\n\t"
      "v_mfma_f32_16x16x32_bf16 v[152:155], a[48:51], a[16:19], v[116:119]\n\t"
      "ds_read_b64_tr_b16 a[84:85], v9 offset:8256\n\t"
      "v_cvt_pk_bf16_f32 v196, v124, v125\n\t"
      "v_mfma_f32_16x16x32_bf16 v[152:155], a[52:55], a[20:23], v[152:155]\n\t"
      "v_exp_f32_e32 v126, v126\n\t"
      "v_mfma_f32_16x16x32_bf16 v[152:155], a[56:59], a[24:27], v[152:155]\n\t"
      "ds_read_b64_tr_b16 a[86:87], v9 offset:12384\n\t"
      "v_mfma_f32_16x16x32_bf16 v[152:155], a[60:63], a[28:31], v[152:155]\n\t"
      "s_memrealtime s[50:51]\n\t"
      "v_exp_f32_e32 v127, v127\n\t"
      "v_mfma_f32_16x16x32_bf16 v[156:159], a[48:51], a[32:35], v[120:123]\n\t"
      "ds_read_b64_tr_b16 a[88:89], v9 offset:8\n\t"
      "v_cvt_pk_bf16_f32 v197, v126, v127\n\t"
      "v_mfma_f32_16x16x32_bf16 v[156:159], a[52:55], a[36:39], v[156:159]\n\t"
      "ds_read_b64_tr_b16 a[90:91], v9 offset:4136\n\t"
      "v_mfma_f32_16x16x32_bf16 v[156:159], a[56:59], a[40:43], v[156:159]\n\t"
      "v_exp_f32_e32 v136, v136\n\t"
      "v_mfma_f32_16x16x32_bf16 v[156:159], a[60:63], a[44:47], v[156:159]\n\t"
      "ds_read_b64_tr_b16 a[92:93], v9 offset:8264\n\t"
      "v_mfma_f32_16x16x32_bf16 v[160:163], a[64:67], a[0:3], v[112:115]\n\t"
      "s_waitcnt lgkmcnt(3)\n\t"
      "v_exp_f32_e32 v137, v137\n\t"
      "v_mfma_f32_16x16x32_bf16 v[160:163], a[68:71], a[4:7], v[160:163]\n\t"
      "ds_read_b64_tr_b16 a[94:95], v9 offset:12392\n\t"
      "v_cvt_pk_bf16_f32 v198, v136, v137\n\t"
      "v_mfma_f32_16x16x32_bf16 v[160:163], a[72:75], a[8:11], v[160:163]\n\t"
      "s_sub_u32 s54, s50, s52\n\t"
      "v_exp_f32_e32 v138, v138\n\t"
      "v_mfma_f32_16x16x32_bf16 v[160:163], a[76:79], a[12:15], v[160:163]\n\t"
      "ds_read_b64_tr_b16 a[96:97], v223 offset:0\n\t"
      "v_mfma_f32_16x16x32_bf16 v[164:167], a[64:67], a[16:19], v[116:119]\n\t"
      "s_cmp_lt_i32 s54, 0\n\t"
      "v_exp_f32_e32 v139, v139\n\t"
      "v_mfma_f32_16x16x32_bf16 v[164:167], a[68:71], a[20:23], v[164:167]\n\t"
      "ds_read_b64_tr_b16 a[98:99], v223 offset:4128\n\t"
      "v_cvt_pk_bf16_f32 v199, v138, v139\n\t"
      "v_mfma_f32_16x16x32_bf16 v[164:167], a[72:75], a[24:27], v[164:167]\n\t"
      "s_cbranch_scc1 .Lgv10_%=\n\t"
      ".Lgr10_%=:\n\t"
      "v_exp_f32_e32 v128, v128\n\t"
      "v_mfma_f32_16x16x32_bf16 v[164:167], a[76:79], a[28:31], v[164:167]\n\t"
      "ds_read_b64_tr_b16 a[100:101], v223 offset:8256\n\t"
      "v_mfma_f32_16x16x32_bf16 v[168:171], a[64:67], a[32:35], v[120:123]\n\t"
      "v_exp_f32_e32 v129, v129\n\t"
      "v_mfma_f32_16x16x32_bf16 v[168:171], a[68:71], a[36:39], v[168:171]\n\t"
      "ds_read_b64_tr_b16 a[102:103], v223 offset:12384\n\t"
      "v_cvt_pk_bf16_f32 v200, v128, v129\n\t"
      "v_mfma_f32_16x16x32_bf16 v[168:171], a[72:75], a[40:43], v[168:171]\n\t"
      "v_exp_f32_e32 v130, v130\n\t"
      "v_mfma_f32_16x16x32_bf16 v[168:171], a[76:79], a[44:47], v[168:171]\n\t"
      "ds_read_b64_tr_b16 a[104:105], v223 offset:8\n\t"
      "v_mfma_f32_16x16x32_bf16 a[116:119], a[112:115], v[184:187], a[116:119]\n\t"
      ".Lent_%=:\n\t"
      "s_waitcnt vmcnt(8) lgkmcnt(3)\n\t"
      "v_exp_f32_e32 v131, v131\n\t"
      "v_mfma_f32_16x16x32_bf16 a[120:123], a[112:115], v[188:191], a[120:123]\n\t"
      "ds_read_b64_tr_b16 a[106:107], v223 offset:4136\n\t"
      "ds_read_b64_tr_b16 a[108:109], v223 offset:8264\n\t"
      "v_mfma_f32_16x16x32_bf16 a[124:127], a[112:115], v[192:195], a[124:127]\n\t"
      "ds_read_b64_tr_b16 a[110:111], v223 offset:12392\n\t"
      "v_cvt_pk_bf16_f32 v201, v130, v131\n\t"
      "v_mfma_f32_16x16x32_bf16 v[16:19], a[80:83], v[172:175], v[16:19]\n\t"
      "s_barrier\n\t"
      "v_exp_f32_e32 v140, v140\n\t"
      "v_mfma_f32_16x16x32_bf16 v[16:19], a[84:87], v[184:187], v[16:19]\n\t"
      "ds_read_b128 a[48:51], v8 offset:32768\n\t"
      "v_mfma_f32_16x16x32_bf16 v[48:51], a[80:83], v[176:179], v[48:51]\n\t"
      "s_add_u32 m0, s38, 0x8100\n\t"
      "s_sub_u32 s54, s54, 8\n\t"
      "v_exp_f32_e32 v141, v141\n\t"
      "v_mfma_f32_16x16x32_bf16 v[48:51], a[84:87], v[188:191], v[48:51]\n\t"
      "buffer_load_dwordx4 v11, s[24:27], s40 offen nt lds\n\t"
      "v_cvt_pk_bf16_f32 v202, v140, v141\n\t"
      "v_mfma_f32_16x16x32_bf16 v[80:83], a[80:83], v[180:183], v[80:83]\n\t"
      "s_max_i32 s54, s54, 0\n\t"
      "v_exp_f32_e32 v142, v142\n\t"
      "v_mfma_f32_16x16x32_bf16 v[80:83], a[84:87], v[192:195], v[80:83]\n\t"
      "buffer_load_dwordx4 v229, s[24:27], s40 offen offset:1032 nt lds\n\t"
      "v_mfma_f32_16x16x32_bf16 v[20:23], a[88:91], v[172:175], v[20:23]\n\t"
      "s_add_u32 s52, s52, s54\n\t"
      "v_exp_f32_e32 v143, v143\n\t"
      "v_mfma_f32_16x16x32_bf16 v[20:23], a[92:95], v[184:187], v[20:23]\n\t"
      "buffer_load_dwordx4 v230, s[24:27], s40 offen offset:2064 nt lds\n\t"
      "v_cvt_pk_bf16_f32 v203, v142, v143\n\t"
      "v_mfma_f32_16x16x32_bf16 v[52:55], a[88:91], v[176:179], v[52:55]\n\t"
      "v_exp_f32_e32 v132, v132\n\t"
      "v_mfma_f32_16x16x32_bf16 v[52:55], a[92:95], v[188:191], v[52:55]\n\t"
      "buffer_load_dwordx4 v231, s[24:27], s40 offen offset:3096 nt lds\n\t"
      "v_mfma_f32_16x16x32_bf16 v[84:87], a[88:91], v[180:183], v[84:87]\n\t"
      "s_add_u32 s52, s52, s53\n\t"
      "v_exp_f32_e32 v133, v133\n\t"
      "v_mfma_f32_16x16x32_bf16 v[84:87], a[92:95], v[192:195], v[84:87]\n\t"
      "ds_read_b128 a[52:55], v220 offset:32768\n\t"
      "v_cvt_pk_bf16_f32 v204, v132, v133\n\t"
      "v_mfma_f32_16x16x32_bf16 v[24:27], a[96:99], v[172:175], v[24:27]\n\t"
      "s_waitcnt lgkmcnt(1)\n\t"
      "v_exp_f32_e32 v134, v134\n\t"
      "v_mfma_f32_16x16x32_bf16 v[24:27], a[100:103], v[184:187], v[24:27]\n\t"
      "ds_read_b128 a[56:59], v221 offset:32768\n\t"
      "v_mfma_f32_16x16x32_bf16 v[56:59], a[96:99], v[176:179], v[56:59]\n\t"
      "s_sub_u32 s53, s53, 1\n\t"
      "v_exp_f32_e32 v135, v135\n\t"
      "v_mfma_f32_16x16x32_bf16 v[56:59], a[100:103], v[188:191], v[56:59]\n\t"
      "ds_read_b128 a[60:63], v222 offset:32768\n\t"
      "v_cvt_pk_bf16_f32 v205, v134, v135\n\t"
      "v_mfma_f32_16x16x32_bf16 v[88:91], a[96:99], v[180:183], v[88:91]\n\t"
      "ds_read_b128 a[64:67], v8 offset:36864\n\t"
      "v_mfma_f32_16x16x32_bf16 v[88:91], a[100:103], v[192:195], v[88:91]\n\t"
      "s_max_i32 s53, s53, s56\n\t"
      "v_exp_f32_e32 v144, v144\n\t"
      "v_mfma_f32_16x16x32_bf16 v[28:31], a[104:107], v[172:175], v[28:31]\n\t"
      "ds_read_b128 a[68:71], v220 offset:36864\n\t"
      "v_mfma_f32_16x16x32_bf16 v[28:31], a[108:111], v[184:187], v[28:31]\n\t"
      "s_waitcnt lgkmcnt(2)\n\t"
      "v_exp_f32_e32 v145, v145\n\t"
      "v_mfma_f32_16x16x32_bf16 v[60:63], a[104:107], v[176:179], v[60:63]\n\t"
      "ds_read_b128 a[72:75], v221 offset:36864\n\t"
      "v_cvt_pk_bf16_f32 v206, v144, v145\n\t"
      "v_mfma_f32_16x16x32_bf16 v[60:63], a[108:111], v[188:191], v[60:63]\n\t"
      "s_cmp_ge_u32 s40, 0x3c000\n\t"
      "v_exp_f32_e32 v146, v146\n\t"
      "v_mfma_f32_16x16x32_bf16 v[92:95], a[104:107], v[180:183], v[92:95]\n\t"
      "ds_read_b128 a[76:79], v222 offset:36864\n\t"
      "v_mfma_f32_16x16x32_bf16 v[92:95], a[108:111], v[192:195], v[92:95]\n\t"
      "s_cbranch_scc1 .Lvk_%=\n\t"
      ".Lvb_%=:\n\t"
      "v_exp_f32_e32 v147, v147\n\t"
      "v_mfma_f32_16x16x32_bf16 v[124:127], a[48:51], a[0:3], v[112:115]\n\t"
      "ds_read_b64_tr_b16 a[80:81], v224 offset:0\n\t"
      "v_cvt_pk_bf16_f32 v207, v146, v147\n\t"
      "v_mfma_f32_16x16x32_bf16 v[124:127], a[52:55], a[4:7], v[124:127]\n\t"
      "s_add_u32 s40, s40, 0x4000\n\t"
      "v_exp_f32_e32 v148, v148\n\t"
      "v_mfma_f32_16x16x32_bf16 v[124:127], a[56:59], a[8:11], v[124:127]\n\t"
      "ds_read_b64_tr_b16 a[82:83], v224 offset:4128\n\t"
      "v_mfma_f32_16x16x32_bf16 v[124:127], a[60:63], a[12:15], v[124:127]\n\t"
      "s_cbranch_vccnz .Lrv3_%=\n\t"
      ".Lrr3_%=:\n\t"
      "v_exp_f32_e32 v149, v149\n\t"
      "v_mfma_f32_16x16x32_bf16 v[128:131], a[48:51], a[16:19], v[116:119]\n\t"
      "ds_read_b64_tr_b16 a[84:85], v224 offset:8256\n\t"
      "v_cvt_pk_bf16_f32 v208, v148, v149\n\t"
      "v_mfma_f32_16x16x32_bf16 v[128:131], a[52:55], a[20:23], v[128:131]\n\t"
      "v_exp_f32_e32 v150, v150\n\t"
      "v_mfma_f32_16x16x32_bf16 v[128:131], a[56:59], a[24:27], v[128:131]\n\t"
      "ds_read_b64_tr_b16 a[86:87], v224 offset:12384\n\t"
      "v_mfma_f32_16x16x32_bf16 v[128:131], a[60:63], a[28:31], v[128:131]\n\t"
      "s_memrealtime s[50:51]\n\t"
      "v_exp_f32_e32 v151, v151\n\t"
      "v_mfma_f32_16x16x32_bf16 v[132:135], a[48:51], a[32:35], v[120:123]\n\t"
      "ds_read_b64_tr_b16 a[88:89], v224 offset:8\n\t"
      "v_cvt_pk_bf16_f32 v209, v150, v151\n\t"
      "v_mfma_f32_16x16x32_bf16 v[132:135], a[52:55], a[36:39], v[132:135]\n\t"
      "ds_read_b64_tr_b16 a[90:91], v224 offset:4136\n\t"
      "v_mfma_f32_16x16x32_bf16 v[132:135], a[56:59], a[40:43], v[132:135]\n\t"
      "s_waitcnt lgkmcnt(3)\n\t"
      "v_exp_f32_e32 v160, v160\n\t"
      "v_mfma_f32_16x16x32_bf16 v[132:135], a[60:63], a[44:47], v[132:135]\n\t"
      "ds_read_b64_tr_b16 a[92:93], v224 offset:8264\n\t"
      "v_mfma_f32_16x16x32_bf16 v[136:139], a[64:67], a[0:3], v[112:115]\n\t"
      "v_exp_f32_e32 v161, v161\n\t"
      "v_mfma_f32_16x16x32_bf16 v[136:139], a[68:71], a[4:7], v[136:139]\n\t"
      "ds_read_b64_tr_b16 a[94:95], v224 offset:12392\n\t"
      "v_cvt_pk_bf16_f32 v210, v160, v161\n\t"
      "v_mfma_f32_16x16x32_bf16 v[136:139], a[72:75], a[8:11], v[136:139]\n\t"
      "s_sub_u32 s54, s50, s52\n\t"
      "v_exp_f32_e32 v162, v162\n\t"
      "v_mfma_f32_16x16x32_bf16 v[136:139], a[76:79], a[12:15], v[136:139]\n\t"
      "ds_read_b64_tr_b16 a[96:97], v225 offset:0\n\t"
      "v_mfma_f32_16x16x32_bf16 v[140:143], a[64:67], a[16:19], v[116:119]\n\t"
      "s_cmp_lt_i32 s54, 0\n\t"
      "v_exp_f32_e32 v163, v163\n\t"
      "v_mfma_f32_16x16x32_bf16 v[140:143], a[68:71], a[20:23], v[140:143]\n\t"
      "ds_read_b64_tr_b16 a[98:99], v225 offset:4128\n\t"
      "v_cvt_pk_bf16_f32 v211, v162, v163\n\t"
      "v_mfma_f32_16x16x32_bf16 v[140:143], a[72:75], a[24:27], v[140:143]\n\t"
      "s_cbranch_scc1 .Lgv3_%=\n\t"
      ".Lgr3_%=:\n\t"
      "v_exp_f32_e32 v152, v152\n\t"
      "v_mfma_f32_16x16x32_bf16 v[140:143], a[76:79], a[28:31], v[140:143]\n\t"
      "ds_read_b64_tr_b16 a[100:101], v225 offset:8256\n\t"
      "v_mfma_f32_16x16x32_bf16 v[144:147], a[64:67], a[32:35], v[120:123]\n\t"
      "v_exp_f32_e32 v153, v153\n\t"
      "v_mfma_f32_16x16x32_bf16 v[144:147], a[68:71], a[36:39], v[144:147]\n\t"
      "ds_read_b64_tr_b16 a[102:103], v225 offset:12384\n\t"
      "v_cvt_pk_bf16_f32 v212, v152, v153\n\t"
      "v_mfma_f32_16x16x32_bf16 v[144:147], a[72:75], a[40:43], v[144:147]\n\t"
      "v_exp_f32_e32 v154, v154\n\t"
      "v_mfma_f32_16x16x32_bf16 v[144:147], a[76:79], a[44:47], v[144:147]\n\t"
      "ds_read_b64_tr_b16 a[104:105], v225 offset:8\n\t"
      "v_mfma_f32_16x16x32_bf16 a[116:119], a[112:115], v[196:199], a[116:119]\n\t"
      "s_waitcnt vmcnt(8) lgkmcnt(3)\n\t"
      "v_exp_f32_e32 v155, v155\n\t"
      "v_mfma_f32_16x16x32_bf16 a[120:123], a[112:115], v[200:203], a[120:123]\n\t"
      "ds_read_b64_tr_b16 a[106:107], v225 offset:4136\n\t"
      "ds_read_b64_tr_b16 a[108:109], v225 offset:8264\n\t"
      "v_mfma_f32_16x16x32_bf16 a[124:127], a[112:115], v[204:207], a[124:127]\n\t"
      "ds_read_b64_tr_b16 a[110:111], v225 offset:12392\n\t"
      "v_cvt_pk_bf16_f32 v213, v154, v155\n\t"
      "v_mfma_f32_16x16x32_bf16 v[32:35], a[80:83], v[172:175], v[32:35]\n\t"
      "s_barrier\n\t"
      "v_exp_f32_e32 v164, v164\n\t"
      "v_mfma_f32_16x16x32_bf16 v[32:35], a[84:87], v[184:187], v[32:35]\n\t"
      "ds_read_b128 a[48:51], v8 offset:40960\n\t"
      "v_mfma_f32_16x16x32_bf16 v[64:67], a[80:83], v[176:179], v[64:67]\n\t"
      "s_add_u32 m0, s37, 0x0\n\t"
      "s_sub_u32 s54, s54, 8\n\t"
      "v_exp_f32_e32 v165, v165\n\t"
      "v_mfma_f32_16x16x32_bf16 v[64:67], a[84:87], v[188:191], v[64:67]\n\t"
      "buffer_load_dwordx4 v10, s[20:23], s40 offen nt lds\n\t"
      "v_cvt_pk_bf16_f32 v214, v164, v165\n\t"
      "v_mfma_f32_16x16x32_bf16 v[96:99], a[80:83], v[180:183], v[96:99]\n\t"
      "s_max_i32 s54, s54, 0\n\t"
      "v_exp_f32_e32 v166, v166\n\t"
      "v_mfma_f32_16x16x32_bf16 v[96:99], a[84:87], v[192:195], v[96:99]\n\t"
      "buffer_load_dwordx4 v226, s[20:23], s40 offen offset:1024 nt lds\n\t"
      "v_mfma_f32_16x16x32_bf16 v[36:39], a[88:91], v[172:175], v[36:39]\n\t"
      "s_add_u32 s52, s52, s54\n\t"
      "v_exp_f32_e32 v167, v167\n\t"
      "v_mfma_f32_16x16x32_bf16 v[36:39], a[92:95], v[184:187], v[36:39]\n\t"
      "buffer_load_dwordx4 v227, s[20:23], s40 offen offset:2048 nt lds\n\t"
      "v_cvt_pk_bf16_f32 v215, v166, v167\n\t"
      "v_mfma_f32_16x16x32_bf16 v[68:71], a[88:91], v[176:179], v[68:71]\n\t"
      "v_exp_f32_e32 v156, v156\n\t"
      "v_mfma_f32_16x16x32_bf16 v[68:71], a[92:95], v[188:191], v[68:71]\n\t"
      "buffer_load_dwordx4 v228, s[20:23], s40 offen offset:3072 nt lds\n\t"
      "v_mfma_f32_16x16x32_bf16 v[100:103], a[88:91], v[180:183], v[100:103]\n\t"
      "s_add_u32 s52, s52, s53\n\t"
      "v_exp_f32_e32 v157, v157\n\t"
      "v_mfma_f32_16x16x32_bf16 v[100:103], a[92:95], v[192:195], v[100:103]\n\t"
      "ds_read_b128 a[52:55], v220 offset:40960\n\t"
      "v_cvt_pk_bf16_f32 v216, v156, v157\n\t"
      "v_mfma_f32_16x16x32_bf16 v[40:43], a[96:99], v[172:175], v[40:43]\n\t"
      "s_waitcnt lgkmcnt(1)\n\t"
      "v_exp_f32_e32 v158, v158\n\t"
      "v_mfma_f32_16x16x32_bf16 v[40:43], a[100:103], v[184:187], v[40:43]\n\t"
      "ds_read_b128 a[56:59], v221 offset:40960\n\t"
      "v_mfma_f32_16x16x32_bf16 v[72:75], a[96:99], v[176:179], v[72:75]\n\t"
      "s_sub_u32 s53, s53, 1\n\t"
      "v_exp_f32_e32 v159, v159\n\t"
      "v_mfma_f32_16x16x32_bf16 v[72:75], a[100:103], v[188:191], v[72:75]\n\t"
      "ds_read_b128 a[60:63], v222 offset:40960\n\t"
      "v_cvt_pk_bf16_f32 v217, v158, v159\n\t"
      "v_mfma_f32_16x16x32_bf16 v[104:107], a[96:99], v[180:183], v[104:107]\n\t"
      "ds_read_b128 a[64:67], v8 offset:45056\n\t"
      "v_mfma_f32_16x16x32_bf16 v[104:107], a[100:103], v[192:195], v[104:107]\n\t"
      "s_max_i32 s53, s53, s56\n\t"
      "v_exp_f32_e32 v168, v168\n\t"
      "v_mfma_f32_16x16x32_bf16 v[44:47], a[104:107], v[172:175], v[44:47]\n\t"
      "ds_read_b128 a[68:71], v220 offset:45056\n\t"
      "v_mfma_f32_16x16x32_bf16 v[44:47], a[108:111], v[184:187], v[44:47]\n\t"
      "v_exp_f32_e32 v169, v169\n\t"
      "v_mfma_f32_16x16x32_bf16 v[76:79], a[104:107], v[176:179], v[76:79]\n\t"
      "ds_read_b128 a[72:75], v221 offset:45056\n\t"
      "v_cvt_pk_bf16_f32 v218, v168, v169\n\t"
      "v_mfma_f32_16x16x32_bf16 v[76:79], a[108:111], v[188:191], v[76:79]\n\t"
      "v_exp_f32_e32 v170, v170\n\t"
      "v_mfma_f32_16x16x32_bf16 v[108:111], a[104:107], v[180:183], v[108:111]\n\t"
      "ds_read_b128 a[76:79], v222 offset:45056\n\t"
      "v_mfma_f32_16x16x32_bf16 v[108:111], a[108:111], v[192:195], v[108:111]\n\t"
      "s_waitcnt lgkmcnt(3)\n\t"
      "v_exp_f32_e32 v171, v171\n\t"
      "v_mfma_f32_16x16x32_bf16 v[148:151], a[48:51], a[0:3], v[112:115]\n\t"
      "ds_read_b64_tr_b16 a[80:81], v9 offset:16512\n\t"
      "v_cvt_pk_bf16_f32 v219, v170, v171\n\t"
      "v_mfma_f32_16x16x32_bf16 v[148:151], a[52:55], a[4:7], v[148:151]\n\t"
      "v_exp_f32_e32 v124, v124\n\t"
      "v_mfma_f32_16x16x32_bf16 v[148:151], a[56:59], a[8:11], v[148:151]\n\t"
      "ds_read_b64_tr_b16 a[82:83], v9 offset:20640\n\t"
      "v_mfma_f32_16x16x32_bf16 v[148:151], a[60:63], a[12:15], v[148:151]\n\t"
      "s_cbranch_vccnz .Lrv4_%=\n\t"
      ".Lrr4_%=:\n\t"
      "v_exp_f32_e32 v125, v125\n\t"
      "v_mfma_f32_16x16x32_bf16 v[152:155], a[48:51], a[16:19], v[116:119]\n\t"
      "ds_read_b64_tr_b16 a[84:85], v9 offset:24768\n\t"
      "v_cvt_pk_bf16_f32 v172, v124, v125\n\t"
      "v_mfma_f32_16x16x32_bf16 v[152:155], a[52:55], a[20:23], v[152:155]\n\t"
      "v_exp_f32_e32 v126, v126\n\t"
      "v_mfma_f32_16x16x32_bf16 v[152:155], a[56:59], a[24:27], v[152:155]\n\t"
      "ds_read_b64_tr_b16 a[86:87], v9 offset:28896\n\t"
      "v_mfma_f32_16x16x32_bf16 v[152:155], a[60:63], a[28:31], v[152:155]\n\t"
      "s_memrealtime s[50:51]\n\t"
      "v_exp_f32_e32 v127, v127\n\t"
      "v_mfma_f32_16x16x32_bf16 v[156:159], a[48:51], a[32:35], v[120:123]\n\t"
      "ds_read_b64_tr_b16 a[88:89], v9 offset:16520\n\t"
      "v_cvt_pk_bf16_f32 v173, v126, v127\n\t"
      "v_mfma_f32_16x16x32_bf16 v[156:159], a[52:55], a[36:39], v[156:159]\n\t"
      "ds_read_b64_tr_b16 a[90:91], v9 offset:20648\n\t"
      "v_mfma_f32_16x16x32_bf16 v[156:159], a[56:59], a[40:43], v[156:159]\n\t"
      "v_exp_f32_e32 v136, v136\n\t"
      "v_mfma_f32_16x16x32_bf16 v[156:159], a[60:63], a[44:47], v[156:159]\n\t"
      "ds_read_b64_tr_b16 a[92:93], v9 offset:24776\n\t"
      "v_mfma_f32_16x16x32_bf16 v[160:163], a[64:67], a[0:3], v[112:115]\n\t"
      "s_waitcnt lgkmcnt(3)\n\t"
      "v_exp_f32_e32 v137, v137\n\t"
      "v_mfma_f32_16x16x32_bf16 v[160:163], a[68:71], a[4:7], v[160:163]\n\t"
      "ds_read_b64_tr_b16 a[94:95], v9 offset:28904\n\t"
      "v_cvt_pk_bf16_f32 v174, v136, v137\n\t"
      "v_mfma_f32_16x16x32_bf16 v[160:163], a[72:75], a[8:11], v[160:163]\n\t"
      "s_sub_u32 s54, s50, s52\n\t"
      "v_exp_f32_e32 v138, v138\n\t"
      "v_mfma_f32_16x16x32_bf16 v[160:163], a[76:79], a[12:15], v[160:163]\n\t"
      "ds_read_b64_tr_b16 a[96:97], v223 offset:16512\n\t"
      "v_mfma_f32_16x16x32_bf16 v[164:167], a[64:67], a[16:19], v[116:119]\n\t"
      "s_cmp_lt_i32 s54, 0\n\t"
      "v_exp_f32_e32 v139, v139\n\t"
      "v_mfma_f32_16x16x32_bf16 v[164:167], a[68:71], a[20:23], v[164:167]\n\t"
      "ds_read_b64_tr_b16 a[98:99], v223 offset:20640\n\t"
      "v_cvt_pk_bf16_f32 v175, v138, v139\n\t"
      "v_mfma_f32_16x16x32_bf16 v[164:167], a[72:75], a[24:27], v[164:167]\n\t"
      "s_cbranch_scc1 .Lgv4_%=\n\t"
      ".Lgr4_%=:\n\t"
      "v_exp_f32_e32 v128, v128\n\t"
      "v_mfma_f32_16x16x32_bf16 v[164:167], a[76:79], a[28:31], v[164:167]\n\t"
      "ds_read_b64_tr_b16 a[100:101], v223 offset:24768\n\t"
      "v_mfma_f32_16x16x32_bf16 v[168:171], a[64:67], a[32:35], v[120:123]\n\t"
      "v_exp_f32_e32 v129, v129\n\t"
      "v_mfma_f32_16x16x32_bf16 v[168:171], a[68:71], a[36:39], v[168:171]\n\t"
      "ds_read_b64_tr_b16 a[102:103], v223 offset:28896\n\t"
      "v_cvt_pk_bf16_f32 v176, v128, v129\n\t"
      "v_mfma_f32_16x16x32_bf16 v[168:171], a[72:75], a[40:43], v[168:171]\n\t"
      "v_exp_f32_e32 v130, v130\n\t"
      "v_mfma_f32_16x16x32_bf16 v[168:171], a[76:79], a[44:47], v[168:171]\n\t"
      "ds_read_b64_tr_b16 a[104:105], v223 offset:16520\n\t"
      "v_mfma_f32_16x16x32_bf16 a[116:119], a[112:115], v[208:211], a[116:119]\n\t"
      "s_waitcnt vmcnt(8) lgkmcnt(3)\n\t"
      "v_exp_f32_e32 v131, v131\n\t"
      "v_mfma_f32_16x16x32_bf16 a[120:123], a[112:115], v[212:215], a[120:123]\n\t"
      "ds_read_b64_tr_b16 a[106:107], v223 offset:20648\n\t"
      "ds_read_b64_tr_b16 a[108:109], v223 offset:24776\n\t"
      "v_mfma_f32_16x16x32_bf16 a[124:127], a[112:115], v[216:219], a[124:127]\n\t"
      "ds_read_b64_tr_b16 a[110:111], v223 offset:28904\n\t"
      "v_cvt_pk_bf16_f32 v177, v130, v131\n\t"
      "v_mfma_f32_16x16x32_bf16 v[16:19], a[80:83], v[196:199], v[16:19]\n\t"
      "s_barrier\n\t"
      "v_exp_f32_e32 v140, v140\n\t"
      "v_mfma_f32_16x16x32_bf16 v[16:19], a[84:87], v[208:211], v[16:19]\n\t"
      "ds_read_b128 a[48:51], v8 offset:49152\n\t"
      "v_mfma_f32_16x16x32_bf16 v[48:51], a[80:83], v[200:203], v[48:51]\n\t"
      "s_add_u32 m0, s38, 0xc180\n\t"
      "s_sub_u32 s54, s54, 8\n\t"
      "v_exp_f32_e32 v141, v141\n\t"
      "v_mfma_f32_16x16x32_bf16 v[48:51], a[84:87], v[212:215], v[48:51]\n\t"
      "buffer_load_dwordx4 v11, s[24:27], s40 offen nt lds\n\t"
      "v_cvt_pk_bf16_f32 v178, v140, v141\n\t"
      "v_mfma_f32_16x16x32_bf16 v[80:83], a[80:83], v[204:207], v[80:83]\n\t"
      "s_max_i32 s54, s54, 0\n\t"
      "v_exp_f32_e32 v142, v142\n\t"
      "v_mfma_f32_16x16x32_bf16 v[80:83], a[84:87], v[216:219], v[80:83]\n\t"
      "buffer_load_dwordx4 v229, s[24:27], s40 offen offset:1032 nt lds\n\t"
      "v_mfma_f32_16x16x32_bf16 v[20:23], a[88:91], v[196:199], v[20:23]\n\t"
      "s_add_u32 s52, s52, s54\n\t"
      "v_exp_f32_e32 v143, v143\n\t"
      "v_mfma_f32_16x16x32_bf16 v[20:23], a[92:95], v[208:211], v[20:23]\n\t"
      "buffer_load_dwordx4 v230, s[24:27], s40 offen offset:2064 nt lds\n\t"
      "v_cvt_pk_bf16_f32 v179, v142, v143\n\t"
      "v_mfma_f32_16x16x32_bf16 v[52:55], a[88:91], v[200:203], v[52:55]\n\t"
      "v_exp_f32_e32 v132, v132\n\t"
      "v_mfma_f32_16x16x32_bf16 v[52:55], a[92:95], v[212:215], v[52:55]\n\t"
      "buffer_load_dwordx4 v231, s[24:27], s40 offen offset:3096 nt lds\n\t"
      "v_mfma_f32_16x16x32_bf16 v[84:87], a[88:91], v[204:207], v[84:87]\n\t"
      "s_add_u32 s52, s52, s53\n\t"
      "v_exp_f32_e32 v133, v133\n\t"
      "v_mfma_f32_16x16x32_bf16 v[84:87], a[92:95], v[216:219], v[84:87]\n\t"
      "ds_read_b128 a[52:55], v220 offset:49152\n\t"
      "v_cvt_pk_bf16_f32 v180, v132, v133\n\t"
      "v_mfma_f32_16x16x32_bf16 v[24:27], a[96:99], v[196:199], v[24:27]\n\t"
      "s_waitcnt lgkmcnt(1)\n\t"
      "v_exp_f32_e32 v134, v134\n\t"
      "v_mfma_f32_16x16x32_bf16 v[24:27], a[100:103], v[208:211], v[24:27]\n\t"
      "ds_read_b128 a[56:59], v221 offset:49152\n\t"
      "v_mfma_f32_16x16x32_bf16 v[56:59], a[96:99], v[200:203], v[56:59]\n\t"
      "s_sub_u32 s53, s53, 1\n\t"
      "v_exp_f32_e32 v135, v135\n\t"
      "v_mfma_f32_16x16x32_bf16 v[56:59], a[100:103], v[212:215], v[56:59]\n\t"
      "ds_read_b128 a[60:63], v222 offset:49152\n\t"
      "v_cvt_pk_bf16_f32 v181, v134, v135\n\t"
      "v_mfma_f32_16x16x32_bf16 v[88:91], a[96:99], v[204:207], v[88:91]\n\t"
      "ds_read_b128 a[64:67], v8 offset:53248\n\t"
      "v_mfma_f32_16x16x32_bf16 v[88:91], a[100:103], v[216:219], v[88:91]\n\t"
      "s_max_i32 s53, s53, s56\n\t"
      "v_exp_f32_e32 v144, v144\n\t"
      "v_mfma_f32_16x16x32_bf16 v[28:31], a[104:107], v[196:199], v[28:31]\n\t"
      "ds_read_b128 a[68:71], v220 offset:53248\n\t"
      "v_mfma_f32_16x16x32_bf16 v[28:31], a[108:111], v[208:211], v[28:31]\n\t"
      "v_exp_f32_e32 v145, v145\n\t"
      "v_mfma_f32_16x16x32_bf16 v[60:63], a[104:107], v[200:203], v[60:63]\n\t"
      "ds_read_b128 a[72:75], v221 offset:53248\n\t"
      "v_cvt_pk_bf16_f32 v182, v144, v145\n\t"
      "v_mfma_f32_16x16x32_bf16 v[60:63], a[108:111], v[212:215], v[60:63]\n\t"
      "v_exp_f32_e32 v146, v146\n\t"
      "v_mfma_f32_16x16x32_bf16 v[92:95], a[104:107], v[204:207], v[92:95]\n\t"
      "ds_read_b128 a[76:79], v222 offset:53248\n\t"
      "v_mfma_f32_16x16x32_bf16 v[92:95], a[108:111], v[216:219], v[92:95]\n\t"
      "s_waitcnt lgkmcnt(3)\n\t"
      "v_exp_f32_e32 v147, v147\n\t"
      "v_mfma_f32_16x16x32_bf16 v[124:127], a[48:51], a[0:3], v[112:115]\n\t"
      "ds_read_b64_tr_b16 a[80:81], v224 offset:16512\n\t"
      "v_cvt_pk_bf16_f32 v183, v146, v147\n\t"
      "v_mfma_f32_16x16x32_bf16 v[124:127], a[52:55], a[4:7], v[124:127]\n\t"
      "s_add_u32 s40, s40, 0x4000\n\t"
      "v_exp_f32_e32 v148, v148\n\t"
      "v_mfma_f32_16x16x32_bf16 v[124:127], a[56:59], a[8:11], v[124:127]\n\t"
      "ds_read_b64_tr_b16 a[82:83], v224 offset:20640\n\t"
      "v_mfma_f32_16x16x32_bf16 v[124:127], a[60:63], a[12:15], v[124:127]\n\t"
      "s_cbranch_vccnz .Lrv5_%=\n\t"
      ".Lrr5_%=:\n\t"
      "v_exp_f32_e32 v149, v149\n\t"
      "v_mfma_f32_16x16x32_bf16 v[128:131], a[48:51], a[16:19], v[116:119]\n\t"
      "ds_read_b64_tr_b16 a[84:85], v224 offset:24768\n\t"
      "v_cvt_pk_bf16_f32 v184, v148, v149\n\t"
      "v_mfma_f32_16x16x32_bf16 v[128:131], a[52:55], a[20:23], v[128:131]\n\t"
      "v_exp_f32_e32 v150, v150\n\t"
      "v_mfma_f32_16x16x32_bf16 v[128:131], a[56:59], a[24:27], v[128:131]\n\t"
      "ds_read_b64_tr_b16 a[86:87], v224 offset:28896\n\t"
      "v_mfma_f32_16x16x32_bf16 v[128:131], a[60:63], a[28:31], v[128:131]\n\t"
      "s_memrealtime s[50:51]\n\t"
      "v_exp_f32_e32 v151, v151\n\t"
      "v_mfma_f32_16x16x32_bf16 v[132:135], a[48:51], a[32:35], v[120:123]\n\t"
      "ds_read_b64_tr_b16 a[88:89], v224 offset:16520\n\t"
      "v_cvt_pk_bf16_f32 v185, v150, v151\n\t"
      "v_mfma_f32_16x16x32_bf16 v[132:135], a[52:55], a[36:39], v[132:135]\n\t"
      "ds_read_b64_tr_b16 a[90:91], v224 offset:20648\n\t"
      "v_mfma_f32_16x16x32_bf16 v[132:135], a[56:59], a[40:43], v[132:135]\n\t"
      "v_exp_f32_e32 v160, v160\n\t"
      "v_mfma_f32_16x16x32_bf16 v[132:135], a[60:63], a[44:47], v[132:135]\n\t"
      "ds_read_b64_tr_b16 a[92:93], v224 offset:24776\n\t"
      "v_mfma_f32_16x16x32_bf16 v[136:139], a[64:67], a[0:3], v[112:115]\n\t"
      "s_waitcnt lgkmcnt(3)\n\t"
      "v_exp_f32_e32 v161, v161\n\t"
      "v_mfma_f32_16x16x32_bf16 v[136:139], a[68:71], a[4:7], v[136:139]\n\t"
      "ds_read_b64_tr_b16 a[94:95], v224 offset:28904\n\t"
      "v_cvt_pk_bf16_f32 v186, v160, v161\n\t"
      "v_mfma_f32_16x16x32_bf16 v[136:139], a[72:75], a[8:11], v[136:139]\n\t"
      "s_sub_u32 s54, s50, s52\n\t"
      "v_exp_f32_e32 v162, v162\n\t"
      "v_mfma_f32_16x16x32_bf16 v[136:139], a[76:79], a[12:15], v[136:139]\n\t"
      "ds_read_b64_tr_b16 a[96:97], v225 offset:16512\n\t"
      "v_mfma_f32_16x16x32_bf16 v[140:143], a[64:67], a[16:19], v[116:119]\n\t"
      "s_cmp_lt_i32 s54, 0\n\t"
      "v_exp_f32_e32 v163, v163\n\t"
      "v_mfma_f32_16x16x32_bf16 v[140:143], a[68:71], a[20:23], v[140:143]\n\t"
      "ds_read_b64_tr_b16 a[98:99], v225 offset:20640\n\t"
      "v_cvt_pk_bf16_f32 v187, v162, v163\n\t"
      "v_mfma_f32_16x16x32_bf16 v[140:143], a[72:75], a[24:27], v[140:143]\n\t"
      "s_cbranch_scc1 .Lgv5_%=\n\t"
      ".Lgr5_%=:\n\t"
      "v_exp_f32_e32 v152, v152\n\t"
      "v_mfma_f32_16x16x32_bf16 v[140:143], a[76:79], a[28:31], v[140:143]\n\t"
      "ds_read_b64_tr_b16 a[100:101], v225 offset:24768\n\t"
      "v_mfma_f32_16x16x32_bf16 v[144:147], a[64:67], a[32:35], v[120:123]\n\t"
      "v_exp_f32_e32 v153, v153\n\t"
      "v_mfma_f32_16x16x32_bf16 v[144:147], a[68:71], a[36:39], v[144:147]\n\t"
      "ds_read_b64_tr_b16 a[102:103], v225 offset:28896\n\t"
      "v_cvt_pk_bf16_f32 v188, v152, v153\n\t"
      "v_mfma_f32_16x16x32_bf16 v[144:147], a[72:75], a[40:43], v[144:147]\n\t"
      "v_exp_f32_e32 v154, v154\n\t"
      "v_mfma_f32_16x16x32_bf16 v[144:147], a[76:79], a[44:47], v[144:147]\n\t"
      "ds_read_b64_tr_b16 a[104:105], v225 offset:16520\n\t"
      "v_mfma_f32_16x16x32_bf16 a[116:119], a[112:115], v[172:175], a[116:119]\n\t"
      "s_waitcnt vmcnt(8) lgkmcnt(3)\n\t"
      "v_exp_f32_e32 v155, v155\n\t"
      "v_mfma_f32_16x16x32_bf16 a[120:123], a[112:115], v[176:179], a[120:123]\n\t"
      "ds_read_b64_tr_b16 a[106:107], v225 offset:20648\n\t"
      "ds_read_b64_tr_b16 a[108:109], v225 offset:24776\n\t"
      "v_mfma_f32_16x16x32_bf16 a[124:127], a[112:115], v[180:183], a[124:127]\n\t"
      "ds_read_b64_tr_b16 a[110:111], v225 offset:28904\n\t"
      "v_cvt_pk_bf16_f32 v189, v154, v155\n\t"
      "v_mfma_f32_16x16x32_bf16 v[32:35], a[80:83], v[196:199], v[32:35]\n\t"
      "s_barrier\n\t"
      "v_exp_f32_e32 v164, v164\n\t"
      "v_mfma_f32_16x16x32_bf16 v[32:35], a[84:87], v[208:211], v[32:35]\n\t"
      "ds_read_b128 a[48:51], v8 offset:57344\n\t"
      "v_mfma_f32_16x16x32_bf16 v[64:67], a[80:83], v[200:203], v[64:67]\n\t"
      "s_add_u32 m0, s37, 0x4000\n\t"
      "s_sub_u32 s54, s54, 8\n\t"
      "v_exp_f32_e32 v165, v165\n\t"
      "v_mfma_f32_16x16x32_bf16 v[64:67], a[84:87], v[212:215], v[64:67]\n\t"
      "buffer_load_dwordx4 v10, s[20:23], s40 offen nt lds\n\t"
      "v_cvt_pk_bf16_f32 v190, v164, v165\n\t"
      "v_mfma_f32_16x16x32_bf16 v[96:99], a[80:83], v[204:207], v[96:99]\n\t"
      "s_max_i32 s54, s54, 0\n\t"
      "v_exp_f32_e32 v166, v166\n\t"
      "v_mfma_f32_16x16x32_bf16 v[96:99], a[84:87], v[216:219], v[96:99]\n\t"
      "buffer_load_dwordx4 v226, s[20:23], s40 offen offset:1024 nt lds\n\t"
      "v_mfma_f32_16x16x32_bf16 v[36:39], a[88:91], v[196:199], v[36:39]\n\t"
      "s_add_u32 s52, s52, s54\n\t"
      "v_exp_f32_e32 v167, v167\n\t"
      "v_mfma_f32_16x16x32_bf16 v[36:39], a[92:95], v[208:211], v[36:39]\n\t"
      "buffer_load_dwordx4 v227, s[20:23], s40 offen offset:2048 nt lds\n\t"
      "v_cvt_pk_bf16_f32 v191, v166, v167\n\t"
      "v_mfma_f32_16x16x32_bf16 v[68:71], a[88:91], v[200:203], v[68:71]\n\t"
      "v_exp_f32_e32 v156, v156\n\t"
      "v_mfma_f32_16x16x32_bf16 v[68:71], a[92:95], v[212:215], v[68:71]\n\t"
      "buffer_load_dwordx4 v228, s[20:23], s40 offen offset:3072 nt lds\n\t"
      "v_mfma_f32_16x16x32_bf16 v[100:103], a[88:91], v[204:207], v[100:103]\n\t"
      "s_add_u32 s52, s52, s53\n\t"
      "v_exp_f32_e32 v157, v157\n\t"
      "v_mfma_f32_16x16x32_bf16 v[100:103], a[92:95], v[216:219], v[100:103]\n\t"
      "ds_read_b128 a[52:55], v220 offset:57344\n\t"
      "v_cvt_pk_bf16_f32 v192, v156, v157\n\t"
      "v_mfma_f32_16x16x32_bf16 v[40:43], a[96:99], v[196:199], v[40:43]\n\t"
      "s_waitcnt lgkmcnt(1)\n\t"
      "v_exp_f32_e32 v158, v158\n\t"
      "v_mfma_f32_16x16x32_bf16 v[40:43], a[100:103], v[208:211], v[40:43]\n\t"
      "ds_read_b128 a[56:59], v221 offset:57344\n\t"
      "v_mfma_f32_16x16x32_bf16 v[72:75], a[96:99], v[200:203], v[72:75]\n\t"
      "s_sub_u32 s53, s53, 1\n\t"
      "v_exp_f32_e32 v159, v159\n\t"
      "v_mfma_f32_16x16x32_bf16 v[72:75], a[100:103], v[212:215], v[72:75]\n\t"
      "ds_read_b128 a[60:63], v222 offset:57344\n\t"
      "v_cvt_pk_bf16_f32 v193, v158, v159\n\t"
      "v_mfma_f32_16x16x32_bf16 v[104:107], a[96:99], v[204:207], v[104:107]\n\t"
      "ds_read_b128 a[64:67], v8 offset:61440\n\t"
      "v_mfma_f32_16x16x32_bf16 v[104:107], a[100:103], v[216:219], v[104:107]\n\t"
      "s_max_i32 s53, s53, s56\n\t"
      "v_exp_f32_e32 v168, v168\n\t"
      "v_mfma_f32_16x16x32_bf16 v[44:47], a[104:107], v[196:199], v[44:47]\n\t"
      "ds_read_b128 a[68:71], v220 offset:61440\n\t"
      "v_mfma_f32_16x16x32_bf16 v[44:47], a[108:111], v[208:211], v[44:47]\n\t"
      "v_exp_f32_e32 v169, v169\n\t"
      "v_mfma_f32_16x16x32_bf16 v[76:79], a[104:107], v[200:203], v[76:79]\n\t"
      "ds_read_b128 a[72:75], v221 offset:61440\n\t"
      "v_cvt_pk_bf16_f32 v194, v168, v169\n\t"
      "v_mfma_f32_16x16x32_bf16 v[76:79], a[108:111], v[212:215], v[76:79]\n\t"
      "v_exp_f32_e32 v170, v170\n\t"
      "v_mfma_f32_16x16x32_bf16 v[108:111], a[104:107], v[204:207], v[108:111]\n\t"
      "ds_read_b128 a[76:79], v222 offset:61440\n\t"
      "v_mfma_f32_16x16x32_bf16 v[108:111], a[108:111], v[216:219], v[108:111]\n\t"
      "s_waitcnt lgkmcnt(3)\n\t"
      "v_exp_f32_e32 v171, v171\n\t"
      "v_mfma_f32_16x16x32_bf16 v[148:151], a[48:51], a[0:3], v[112:115]\n\t"
      "ds_read_b64_tr_b16 a[80:81], v9 offset:33024\n\t"
      "v_cvt_pk_bf16_f32 v195, v170, v171\n\t"
      "v_mfma_f32_16x16x32_bf16 v[148:151], a[52:55], a[4:7], v[148:151]\n\t"
      "v_exp_f32_e32 v124, v124\n\t"
      "v_mfma_f32_16x16x32_bf16 v[148:151], a[56:59], a[8:11], v[148:151]\n\t"
      "ds_read_b64_tr_b16 a[82:83], v9 offset:37152\n\t"
      "v_mfma_f32_16x16x32_bf16 v[148:151], a[60:63], a[12:15], v[148:151]\n\t"
      "s_cbranch_vccnz .Lrv6_%=\n\t"
      ".Lrr6_%=:\n\t"
      "v_exp_f32_e32 v125, v125\n\t"
      "v_mfma_f32_16x16x32_bf16 v[152:155], a[48:51], a[16:19], v[116:119]\n\t"
      "ds_read_b64_tr_b16 a[84:85], v9 offset:41280\n\t"
      "v_cvt_pk_bf16_f32 v196, v124, v125\n\t"
      "v_mfma_f32_16x16x32_bf16 v[152:155], a[52:55], a[20:23], v[152:155]\n\t"
      "v_exp_f32_e32 v126, v126\n\t"
      "v_mfma_f32_16x16x32_bf16 v[152:155], a[56:59], a[24:27], v[152:155]\n\t"
      "ds_read_b64_tr_b16 a[86:87], v9 offset:45408\n\t"
      "v_mfma_f32_16x16x32_bf16 v[152:155], a[60:63], a[28:31], v[152:155]\n\t"
      "s_memrealtime s[50:51]\n\t"
      "v_exp_f32_e32 v127, v127\n\t"
      "v_mfma_f32_16x16x32_bf16 v[156:159], a[48:51], a[32:35], v[120:123]\n\t"
      "ds_read_b64_tr_b16 a[88:89], v9 offset:33032\n\t"
      "v_cvt_pk_bf16_f32 v197, v126, v127\n\t"
      "v_mfma_f32_16x16x32_bf16 v[156:159], a[52:55], a[36:39], v[156:159]\n\t"
      "ds_read_b64_tr_b16 a[90:91], v9 offset:37160\n\t"
      "v_mfma_f32_16x16x32_bf16 v[156:159], a[56:59], a[40:43], v[156:159]\n\t"
      "v_exp_f32_e32 v136, v136\n\t"
      "v_mfma_f32_16x16x32_bf16 v[156:159], a[60:63], a[44:47], v[156:159]\n\t"
      "ds_read_b64_tr_b16 a[92:93], v9 offset:41288\n\t"
      "v_mfma_f32_16x16x32_bf16 v[160:163], a[64:67], a[0:3], v[112:115]\n\t"
      "s_waitcnt lgkmcnt(3)\n\t"
      "v_exp_f32_e32 v137, v137\n\t"
      "v_mfma_f32_16x16x32_bf16 v[160:163], a[68:71], a[4:7], v[160:163]\n\t"
      "ds_read_b64_tr_b16 a[94:95], v9 offset:45416\n\t"
      "v_cvt_pk_bf16_f32 v198, v136, v137\n\t"
      "v_mfma_f32_16x16x32_bf16 v[160:163], a[72:75], a[8:11], v[160:163]\n\t"
      "s_sub_u32 s54, s50, s52\n\t"
      "v_exp_f32_e32 v138, v138\n\t"
      "v_mfma_f32_16x16x32_bf16 v[160:163], a[76:79], a[12:15], v[160:163]\n\t"
      "ds_read_b64_tr_b16 a[96:97], v223 offset:33024\n\t"
      "v_mfma_f32_16x16x32_bf16 v[164:167], a[64:67], a[16:19], v[116:119]\n\t"
      "s_cmp_lt_i32 s54, 0\n\t"
      "v_exp_f32_e32 v139, v139\n\t"
      "v_mfma_f32_16x16x32_bf16 v[164:167], a[68:71], a[20:23], v[164:167]\n\t"
      "ds_read_b64_tr_b16 a[98:99], v223 offset:37152\n\t"
      "v_cvt_pk_bf16_f32 v199, v138, v139\n\t"
      "v_mfma_f32_16x16x32_bf16 v[164:167], a[72:75], a[24:27], v[164:167]\n\t"
      "s_cbranch_scc1 .Lgv6_%=\n\t"
      ".Lgr6_%=:\n\t"
      "v_exp_f32_e32 v128, v128\n\t"
      "v_mfma_f32_16x16x32_bf16 v[164:167], a[76:79], a[28:31], v[164:167]\n\t"
      "ds_read_b64_tr_b16 a[100:101], v223 offset:41280\n\t"
      "v_mfma_f32_16x16x32_bf16 v[168:171], a[64:67], a[32:35], v[120:123]\n\t"
      "s_cmp_lt_u32 s40, 0x44000\n\t"
      "v_exp_f32_e32 v129, v129\n\t"
      "v_mfma_f32_16x16x32_bf16 v[168:171], a[68:71], a[36:39], v[168:171]\n\t"
      "ds_read_b64_tr_b16 a[102:103], v223 offset:45408\n\t"
      "v_cvt_pk_bf16_f32 v200, v128, v129\n\t"
      "v_mfma_f32_16x16x32_bf16 v[168:171], a[72:75], a[40:43], v[168:171]\n\t"
      "v_exp_f32_e32 v130, v130\n\t"
      "v_mfma_f32_16x16x32_bf16 v[168:171], a[76:79], a[44:47], v[168:171]\n\t"
      "ds_read_b64_tr_b16 a[104:105], v223 offset:33032\n\t"
      "v_mfma_f32_16x16x32_bf16 a[116:119], a[112:115], v[184:187], a[116:119]\n\t"
      "s_cbranch_scc1 .Lloop_%=\n\t"
      "s_mov_b32 s42, 0x10000\n\t"
      "v_bfe_u32 v241, %[tid], 4, 2\n\t"
      "v_lshl_add_u32 v241, v241, 3, s42\n\t"
      "v_sub_u32_e32 v8, v9, v241\n\t"
      "v_sub_u32_e32 v220, v223, v241\n\t"
      "v_mfma_f32_16x16x32_bf16 a[120:123], a[112:115], v[188:191], a[120:123]\n\t"
      "ds_read_b64_tr_b16 a[106:107], v223 offset:37160\n\t"
      "v_sub_u32_e32 v221, v224, v241\n\t"
      "v_sub_u32_e32 v222, v225, v241\n\t"
      "v_exp_f32_e32 v131, v131\n\t"
      "v_mfma_f32_16x16x32_bf16 a[124:127], a[112:115], v[192:195], a[124:127]\n\t"
      "ds_read_b64_tr_b16 a[108:109], v223 offset:41288\n\t"
      "v_cvt_pk_bf16_f32 v201, v130, v131\n\t"
      "v_exp_f32_e32 v140, v140\n\t"
      "s_waitcnt lgkmcnt(2)\n\t"
      "v_mfma_f32_16x16x32_bf16 v[16:19], a[80:83], v[172:175], v[16:19]\n\t"
      "ds_read_b64_tr_b16 a[110:111], v223 offset:45416\n\t"
      "v_exp_f32_e32 v141, v141\n\t"
      "s_nop 0\n\t"
      "v_cvt_pk_bf16_f32 v202, v140, v141\n\t"
      "v_mfma_f32_16x16x32_bf16 v[16:19], a[84:87], v[184:187], v[16:19]\n\t"
      "ds_read_b64_tr_b16 a[0:1], v224 offset:33024\n\t"
      "v_exp_f32_e32 v142, v142\n\t"
      "v_exp_f32_e32 v143, v143\n\t"
      "v_mfma_f32_16x16x32_bf16 v[48:51], a[80:83], v[176:179], v[48:51]\n\t"
      "ds_read_b64_tr_b16 a[2:3], v224 offset:37152\n\t"
      "v_cvt_pk_bf16_f32 v203, v142, v143\n\t"
      "v_exp_f32_e32 v132, v132\n\t"
      "v_mfma_f32_16x16x32_bf16 v[48:51], a[84:87], v[188:191], v[48:51]\n\t"
      "ds_read_b64_tr_b16 a[4:5], v224 offset:41280\n\t"
      "v_exp_f32_e32 v133, v133\n\t"
      "s_nop 0\n\t"
      "v_cvt_pk_bf16_f32 v204, v132, v133\n\t"
      "v_mfma_f32_16x16x32_bf16 v[80:83], a[80:83], v[180:183], v[80:83]\n\t"
      "ds_read_b64_tr_b16 a[6:7], v224 offset:45408\n\t"
      "v_exp_f32_e32 v134, v134\n\t"
      "v_exp_f32_e32 v135, v135\n\t"
      "v_mfma_f32_16x16x32_bf16 v[80:83], a[84:87], v[192:195], v[80:83]\n\t"
      "ds_read_b64_tr_b16 a[8:9], v224 offset:33032\n\t"
      "v_cvt_pk_bf16_f32 v205, v134, v135\n\t"
      "v_exp_f32_e32 v144, v144\n\t"
      "v_mfma_f32_16x16x32_bf16 v[20:23], a[88:91], v[172:175], v[20:23]\n\t"
      "ds_read_b64_tr_b16 a[10:11], v224 offset:37160\n\t"
      "v_exp_f32_e32 v145, v145\n\t"
      "s_nop 0\n\t"
      "v_cvt_pk_bf16_f32 v206, v144, v145\n\t"
      "v_mfma_f32_16x16x32_bf16 v[20:23], a[92:95], v[184:187], v[20:23]\n\t"
      "ds_read_b64_tr_b16 a[12:13], v224 offset:41288\n\t"
      "v_exp_f32_e32 v146, v146\n\t"
      "v_exp_f32_e32 v147, v147\n\t"
      "v_mfma_f32_16x16x32_bf16 v[52:55], a[88:91], v[176:179], v[52:55]\n\t"
      "s_waitcnt vmcnt(0)\n\t"
      "s_barrier\n\t"
      "s_add_u32 s44, s36, 4096\n\t"
      "s_add_u32 s45, s36, 8192\n\t"
      "v_exp_f32_e32 v148, v148\n\t"
      "v_cvt_pk_bf16_f32 v207, v146, v147\n\t"
      "v_mfma_f32_16x16x32_bf16 v[52:55], a[92:95], v[188:191], v[52:55]\n\t"
      "ds_read_b64_tr_b16 a[48:49], v8 offset:0\n\t"
      "v_exp_f32_e32 v149, v149\n\t"
      "v_exp_f32_e32 v150, v150\n\t"
      "v_mfma_f32_16x16x32_bf16 v[84:87], a[88:91], v[180:183], v[84:87]\n\t"
      "ds_read_b64_tr_b16 a[50:51], v8 offset:4096\n\t"
      "v_cvt_pk_bf16_f32 v208, v148, v149\n\t"
      "v_exp_f32_e32 v151, v151\n\t"
      "v_mfma_f32_16x16x32_bf16 v[84:87], a[92:95], v[192:195], v[84:87]\n\t"
      "ds_read_b64_tr_b16 a[52:53], v8 offset:8192\n\t"
      "v_exp_f32_e32 v160, v160\n\t"
      "v_cvt_pk_bf16_f32 v209, v150, v151\n\t"
      "v_mfma_f32_16x16x32_bf16 v[24:27], a[96:99], v[172:175], v[24:27]\n\t"
      "ds_read_b64_tr_b16 a[54:55], v8 offset:12288\n\t"
      "v_exp_f32_e32 v161, v161\n\t"
      "v_exp_f32_e32 v162, v162\n\t"
      "v_mfma_f32_16x16x32_bf16 v[24:27], a[100:103], v[184:187], v[24:27]\n\t"
      "ds_read_b64_tr_b16 a[56:57], v8 offset:8\n\t"
      "v_cvt_pk_bf16_f32 v210, v160, v161\n\t"
      "v_exp_f32_e32 v163, v163\n\t"
      "v_mfma_f32_16x16x32_bf16 v[56:59], a[96:99], v[176:179], v[56:59]\n\t"
      "ds_read_b64_tr_b16 a[58:59], v8 offset:4104\n\t"
      "v_exp_f32_e32 v152, v152\n\t"
      "v_cvt_pk_bf16_f32 v211, v162, v163\n\t"
      "v_mfma_f32_16x16x32_bf16 v[56:59], a[100:103], v[188:191], v[56:59]\n\t"
      "ds_read_b64_tr_b16 a[60:61], v8 offset:8200\n\t"
      "v_exp_f32_e32 v153, v153\n\t"
      "v_exp_f32_e32 v154, v154\n\t"
      "v_mfma_f32_16x16x32_bf16 v[88:91], a[96:99], v[180:183], v[88:91]\n\t"
      "ds_read_b64_tr_b16 a[62:63], v8 offset:12296\n\t"
      "v_cvt_pk_bf16_f32 v212, v152, v153\n\t"
      "v_exp_f32_e32 v155, v155\n\t"
      "v_mfma_f32_16x16x32_bf16 v[88:91], a[100:103], v[192:195], v[88:91]\n\t"
      "ds_read_b64_tr_b16 a[64:65], v220 offset:0\n\t"
      "v_exp_f32_e32 v164, v164\n\t"
      "v_cvt_pk_bf16_f32 v213, v154, v155\n\t"
      "s_waitcnt lgkmcnt(5)\n\t"
      "v_mfma_f32_16x16x32_bf16 v[28:31], a[104:107], v[172:175], v[28:31]\n\t"
      "ds_read_b64_tr_b16 a[66:67], v220 offset:4096\n\t"
      "v_exp_f32_e32 v165, v165\n\t"
      "v_exp_f32_e32 v166, v166\n\t"
      "v_mfma_f32_16x16x32_bf16 v[28:31], a[108:111], v[184:187], v[28:31]\n\t"
      "ds_read_b64_tr_b16 a[68:69], v220 offset:8192\n\t"
      "v_cvt_pk_bf16_f32 v214, v164, v165\n\t"
      "v_exp_f32_e32 v167, v167\n\t"
      "v_mfma_f32_16x16x32_bf16 v[60:63], a[104:107], v[176:179], v[60:63]\n\t"
      "ds_read_b64_tr_b16 a[70:71], v220 offset:12288\n\t"
      "v_exp_f32_e32 v156, v156\n\t"
      "v_cvt_pk_bf16_f32 v215, v166, v167\n\t"
      "v_mfma_f32_16x16x32_bf16 v[60:63], a[108:111], v[188:191], v[60:63]\n\t"
      "ds_read_b64_tr_b16 a[72:73], v220 offset:8\n\t"
      "v_exp_f32_e32 v157, v157\n\t"
      "v_exp_f32_e32 v158, v158\n\t"
      "v_mfma_f32_16x16x32_bf16 v[92:95], a[104:107], v[180:183], v[92:95]\n\t"
      "ds_read_b64_tr_b16 a[74:75], v220 offset:4104\n\t"
      "v_cvt_pk_bf16_f32 v216, v156, v157\n\t"
      "v_exp_f32_e32 v159, v159\n\t"
      "v_mfma_f32_16x16x32_bf16 v[92:95], a[108:111], v[192:195], v[92:95]\n\t"
      "ds_read_b64_tr_b16 a[76:77], v220 offset:8200\n\t"
      "v_exp_f32_e32 v168, v168\n\t"
      "v_cvt_pk_bf16_f32 v217, v158, v159\n\t"
      "v_mfma_f32_16x16x32_bf16 a[116:119], a[112:115], v[196:199], a[116:119]\n\t"
      "ds_read_b64_tr_b16 a[78:79], v220 offset:12296\n\t"
      "v_exp_f32_e32 v169, v169\n\t"
      "v_exp_f32_e32 v170, v170\n\t"
      "v_mfma_f32_16x16x32_bf16 a[120:123], a[112:115], v[200:203], a[120:123]\n\t"
      "ds_read_b64_tr_b16 a[14:15], v224 offset:45416\n\t"
      "v_cvt_pk_bf16_f32 v218, v168, v169\n\t"
      "v_exp_f32_e32 v171, v171\n\t"
      "v_xor_b32_e32 v240, 64, v12\n\t"
      "v_mfma_f32_16x16x32_bf16 a[124:127], a[112:115], v[204:207], a[124:127]\n\t"
      "ds_read_b64_tr_b16 a[16:17], v225 offset:33024\n\t"
      "v_cvt_pk_bf16_f32 v219, v170, v171\n\t"
      "v_mfma_f32_16x16x32_bf16 a[116:119], a[112:115], v[208:211], a[116:119]\n\t"
      "ds_read_b64_tr_b16 a[18:19], v225 offset:37152\n\t"
      "v_mfma_f32_16x16x32_bf16 a[120:123], a[112:115], v[212:215], a[120:123]\n\t"
      "ds_read_b64_tr_b16 a[20:21], v225 offset:41280\n\t"
      "v_mfma_f32_16x16x32_bf16 a[124:127], a[112:115], v[216:219], a[124:127]\n\t"
      "ds_read_b64_tr_b16 a[22:23], v225 offset:45408\n\t"
      "v_mfma_f32_16x16x32_bf16 v[16:19], a[48:51], v[196:199], v[16:19]\n\t"
      "ds_read_b64_tr_b16 a[24:25], v225 offset:33032\n\t"
      "v_mfma_f32_16x16x32_bf16 v[16:19], a[52:55], v[208:211], v[16:19]\n\t"
      "ds_read_b64_tr_b16 a[26:27], v225 offset:37160\n\t"
      "s_waitcnt lgkmcnt(7)\n\t"
      "v_mfma_f32_16x16x32_bf16 v[20:23], a[56:59], v[196:199], v[20:23]\n\t"
      "ds_read_b64_tr_b16 a[28:29], v225 offset:41288\n\t"
      "v_accvgpr_read_b32 v233, a116\n\t"
      "v_accvgpr_read_b32 v234, a120\n\t"
      "v_accvgpr_read_b32 v235, a124\n\t"
      "v_mfma_f32_16x16x32_bf16 v[20:23], a[60:63], v[208:211], v[20:23]\n\t"
      "ds_read_b64_tr_b16 a[30:31], v225 offset:45416\n\t"
      "v_add_f32_e32 v232, v233, v234\n\t"
      "v_add_f32_e32 v232, v232, v235\n\t"
      "v_mfma_f32_16x16x32_bf16 v[24:27], a[64:67], v[196:199], v[24:27]\n\t"
      "ds_read_b64_tr_b16 a[80:81], v221 offset:0\n\t"
      "v_rcp_f32_e32 v233, v233\n\t"
      "v_mfma_f32_16x16x32_bf16 v[24:27], a[68:71], v[208:211], v[24:27]\n\t"
      "ds_read_b64_tr_b16 a[82:83], v221 offset:4096\n\t"
      "v_rcp_f32_e32 v234, v234\n\t"
      "v_mfma_f32_16x16x32_bf16 v[28:31], a[72:75], v[196:199], v[28:31]\n\t"
      "ds_read_b64_tr_b16 a[84:85], v221 offset:8192\n\t"
      "v_rcp_f32_e32 v235, v235\n\t"
      "v_cmp_nge_f32_e32 vcc, 0x5d800000, v232\n\t"
      "v_mfma_f32_16x16x32_bf16 v[28:31], a[76:79], v[208:211], v[28:31]\n\t"
      "ds_read_b64_tr_b16 a[86:87], v221 offset:12288\n\t"
      "v_mov_b32_e32 v232, 1\n\t"
      "v_mul_f32_e32 v16, v233, v16\n\t"
      "v_mul_f32_e32 v17, v233, v17\n\t"
      "v_mfma_f32_16x16x32_bf16 v[48:51], a[48:51], v[200:203], v[48:51]\n\t"
      "ds_read_b64_tr_b16 a[88:89], v221 offset:8\n\t"
      "v_mul_f32_e32 v18, v233, v18\n\t"
      "v_mul_f32_e32 v19, v233, v19\n\t"
      "v_mul_f32_e32 v20, v233, v20\n\t"
      "v_mfma_f32_16x16x32_bf16 v[48:51], a[52:55], v[212:215], v[48:51]\n\t"
      "ds_read_b64_tr_b16 a[90:91], v221 offset:4104\n\t"
      "v_mul_f32_e32 v21, v233, v21\n\t"
      "v_mul_f32_e32 v22, v233, v22\n\t"
      "v_mul_f32_e32 v23, v233, v23\n\t"
      "v_mfma_f32_16x16x32_bf16 v[52:55], a[56:59], v[200:203], v[52:55]\n\t"
      "ds_read_b64_tr_b16 a[92:93], v221 offset:8200\n\t"
      "v_cvt_pk_bf16_f32 v124, v16, v17\n\t"
      "v_cvt_pk_bf16_f32 v125, v18, v19\n\t"
      "v_cvt_pk_bf16_f32 v126, v20, v21\n\t"
      "v_mfma_f32_16x16x32_bf16 v[52:55], a[60:63], v[212:215], v[52:55]\n\t"
      "ds_read_b64_tr_b16 a[94:95], v221 offset:12296\n\t"
      "v_cvt_pk_bf16_f32 v127, v22, v23\n\t"
      "v_mul_f32_e32 v24, v233, v24\n\t"
      "v_mul_f32_e32 v25, v233, v25\n\t"
      "v_mfma_f32_16x16x32_bf16 v[56:59], a[64:67], v[200:203], v[56:59]\n\t"
      "ds_write_b128 v12, v[124:127] offset:16384\n\t"
      "v_mul_f32_e32 v26, v233, v26\n\t"
      "v_mul_f32_e32 v27, v233, v27\n\t"
      "v_mul_f32_e32 v28, v233, v28\n\t"
      "v_mfma_f32_16x16x32_bf16 v[56:59], a[68:71], v[212:215], v[56:59]\n\t"
      "ds_read_b64_tr_b16 a[96:97], v222 offset:0\n\t"
      "v_mul_f32_e32 v29, v233, v29\n\t"
      "v_mul_f32_e32 v30, v233, v30\n\t"
      "v_mul_f32_e32 v31, v233, v31\n\t"
      "v_mfma_f32_16x16x32_bf16 v[60:63], a[72:75], v[200:203], v[60:63]\n\t"
      "ds_read_b64_tr_b16 a[98:99], v222 offset:4096\n\t"
      "v_cvt_pk_bf16_f32 v128, v24, v25\n\t"
      "v_cvt_pk_bf16_f32 v129, v26, v27\n\t"
      "v_cvt_pk_bf16_f32 v130, v28, v29\n\t"
      "v_mfma_f32_16x16x32_bf16 v[60:63], a[76:79], v[212:215], v[60:63]\n\t"
      "ds_read_b64_tr_b16 a[100:101], v222 offset:8192\n\t"
      "v_cvt_pk_bf16_f32 v131, v30, v31\n\t"
      "v_mul_f32_e32 v48, v234, v48\n\t"
      "v_mul_f32_e32 v49, v234, v49\n\t"
      "v_mfma_f32_16x16x32_bf16 v[80:83], a[48:51], v[204:207], v[80:83]\n\t"
      "ds_write_b128 v240, v[128:131] offset:16384\n\t"
      "v_mul_f32_e32 v50, v234, v50\n\t"
      "v_mul_f32_e32 v51, v234, v51\n\t"
      "v_mul_f32_e32 v52, v234, v52\n\t"
      "v_mfma_f32_16x16x32_bf16 v[80:83], a[52:55], v[216:219], v[80:83]\n\t"
      "ds_read_b128 v[16:19], v13 offset:16384\n\t"
      "v_mul_f32_e32 v53, v234, v53\n\t"
      "v_mul_f32_e32 v54, v234, v54\n\t"
      "v_mul_f32_e32 v55, v234, v55\n\t"
      "v_mfma_f32_16x16x32_bf16 v[84:87], a[56:59], v[204:207], v[84:87]\n\t"
      "ds_read_b128 v[20:23], v13 offset:17408\n\t"
      "v_cvt_pk_bf16_f32 v132, v48, v49\n\t"
      "v_cvt_pk_bf16_f32 v133, v50, v51\n\t"
      "v_cvt_pk_bf16_f32 v134, v52, v53\n\t"
      "v_mfma_f32_16x16x32_bf16 v[84:87], a[60:63], v[216:219], v[84:87]\n\t"
      "ds_read_b64_tr_b16 a[102:103], v222 offset:12288\n\t"
      "v_cvt_pk_bf16_f32 v135, v54, v55\n\t"
      "v_mul_f32_e32 v56, v234, v56\n\t"
      "v_mul_f32_e32 v57, v234, v57\n\t"
      "v_mfma_f32_16x16x32_bf16 v[88:91], a[64:67], v[204:207], v[88:91]\n\t"
      "ds_write_b128 v12, v[132:135] offset:18432\n\t"
      "v_mul_f32_e32 v58, v234, v58\n\t"
      "v_mul_f32_e32 v59, v234, v59\n\t"
      "v_mul_f32_e32 v60, v234, v60\n\t"
      "v_mfma_f32_16x16x32_bf16 v[88:91], a[68:71], v[216:219], v[88:91]\n\t"
      "ds_read_b64_tr_b16 a[104:105], v222 offset:8\n\t"
      "v_mul_f32_e32 v61, v234, v61\n\t"
      "v_mul_f32_e32 v62, v234, v62\n\t"
      "v_mul_f32_e32 v63, v234, v63\n\t"
      "v_mfma_f32_16x16x32_bf16 v[92:95], a[72:75], v[204:207], v[92:95]\n\t"
      "ds_read_b64_tr_b16 a[106:107], v222 offset:4104\n\t"
      "v_cvt_pk_bf16_f32 v136, v56, v57\n\t"
      "v_cvt_pk_bf16_f32 v137, v58, v59\n\t"
      "v_cvt_pk_bf16_f32 v138, v60, v61\n\t"
      "v_mfma_f32_16x16x32_bf16 v[92:95], a[76:79], v[216:219], v[92:95]\n\t"
      "s_waitcnt lgkmcnt(3)\n\t"
      "buffer_store_dwordx4 v[16:19], v14, s[88:91], s36 offen offset:0 sc1\n\t"
      "v_cvt_pk_bf16_f32 v139, v62, v63\n\t"
      "v_mul_f32_e32 v80, v235, v80\n\t"
      "v_mul_f32_e32 v81, v235, v81\n\t"
      "v_mfma_f32_16x16x32_bf16 v[32:35], a[0:3], v[172:175], v[32:35]\n\t"
      "buffer_store_dwordx4 v[20:23], v14, s[88:91], s36 offen offset:2048 sc1\n\t"
      "v_mul_f32_e32 v82, v235, v82\n\t"
      "v_mul_f32_e32 v83, v235, v83\n\t"
      "v_mul_f32_e32 v84, v235, v84\n\t"
      "v_mfma_f32_16x16x32_bf16 v[32:35], a[4:7], v[184:187], v[32:35]\n\t"
      "ds_write_b128 v240, v[136:139] offset:18432\n\t"
      "v_mul_f32_e32 v85, v235, v85\n\t"
      "v_mul_f32_e32 v86, v235, v86\n\t"
      "v_mul_f32_e32 v87, v235, v87\n\t"
      "v_mfma_f32_16x16x32_bf16 v[32:35], a[80:83], v[196:199], v[32:35]\n\t"
      "ds_read_b128 v[48:51], v13 offset:18432\n\t"
      "v_cvt_pk_bf16_f32 v140, v80, v81\n\t"
      "v_cvt_pk_bf16_f32 v141, v82, v83\n\t"
      "v_cvt_pk_bf16_f32 v142, v84, v85\n\t"
      "v_mfma_f32_16x16x32_bf16 v[32:35], a[84:87], v[208:211], v[32:35]\n\t"
      "ds_read_b128 v[52:55], v13 offset:19456\n\t"
      "v_cvt_pk_bf16_f32 v143, v86, v87\n\t"
      "v_mul_f32_e32 v88, v235, v88\n\t"
      "v_mul_f32_e32 v89, v235, v89\n\t"
      "v_mfma_f32_16x16x32_bf16 v[36:39], a[8:11], v[172:175], v[36:39]\n\t"
      "ds_write_b128 v12, v[140:143] offset:20480\n\t"
      "v_mul_f32_e32 v90, v235, v90\n\t"
      "v_mul_f32_e32 v91, v235, v91\n\t"
      "v_mul_f32_e32 v92, v235, v92\n\t"
      "v_mfma_f32_16x16x32_bf16 v[36:39], a[12:15], v[184:187], v[36:39]\n\t"
      "ds_read_b64_tr_b16 a[108:109], v222 offset:8200\n\t"
      "v_mul_f32_e32 v93, v235, v93\n\t"
      "v_mul_f32_e32 v94, v235, v94\n\t"
      "v_mul_f32_e32 v95, v235, v95\n\t"
      "v_mfma_f32_16x16x32_bf16 v[36:39], a[88:91], v[196:199], v[36:39]\n\t"
      "ds_read_b64_tr_b16 a[110:111], v222 offset:12296\n\t"
      "v_cvt_pk_bf16_f32 v144, v88, v89\n\t"
      "v_cvt_pk_bf16_f32 v145, v90, v91\n\t"
      "v_cvt_pk_bf16_f32 v146, v92, v93\n\t"
      "v_mfma_f32_16x16x32_bf16 v[36:39], a[92:95], v[208:211], v[36:39]\n\t"
      "v_cvt_pk_bf16_f32 v147, v94, v95\n\t"
      "v_mul_f32_e32 v32, v233, v32\n\t"
      "v_mul_f32_e32 v33, v233, v33\n\t"
      "v_mfma_f32_16x16x32_bf16 v[40:43], a[16:19], v[172:175], v[40:43]\n\t"
      "s_waitcnt lgkmcnt(3)\n\t"
      "buffer_store_dwordx4 v[48:51], v14, s[88:91], s44 offen offset:0 sc1\n\t"
      "v_mul_f32_e32 v34, v233, v34\n\t"
      "v_mul_f32_e32 v35, v233, v35\n\t"
      "v_mfma_f32_16x16x32_bf16 v[40:43], a[20:23], v[184:187], v[40:43]\n\t"
      "buffer_store_dwordx4 v[52:55], v14, s[88:91], s44 offen offset:2048 sc1\n\t"
      "v_mfma_f32_16x16x32_bf16 v[40:43], a[96:99], v[196:199], v[40:43]\n\t"
      "ds_write_b128 v240, v[144:147] offset:20480\n\t"
      "v_mul_f32_e32 v36, v233, v36\n\t"
      "v_mul_f32_e32 v37, v233, v37\n\t"
      "v_mul_f32_e32 v38, v233, v38\n\t"
      "v_mfma_f32_16x16x32_bf16 v[40:43], a[100:103], v[208:211], v[40:43]\n\t"
      "ds_read_b128 v[80:83], v13 offset:20480\n\t"
      "v_mul_f32_e32 v39, v233, v39\n\t"
      "v_cvt_pk_bf16_f32 v148, v32, v33\n\t"
      "v_cvt_pk_bf16_f32 v149, v34, v35\n\t"
      "v_mfma_f32_16x16x32_bf16 v[44:47], a[24:27], v[172:175], v[44:47]\n\t"
      "ds_read_b128 v[84:87], v13 offset:21504\n\t"
      "v_cvt_pk_bf16_f32 v150, v36, v37\n\t"
      "v_cvt_pk_bf16_f32 v151, v38, v39\n\t"
      "v_mfma_f32_16x16x32_bf16 v[44:47], a[28:31], v[184:187], v[44:47]\n\t"
      "ds_write_b128 v12, v[148:151] offset:22528\n\t"
      "v_mfma_f32_16x16x32_bf16 v[44:47], a[104:107], v[196:199], v[44:47]\n\t"
      "s_waitcnt lgkmcnt(4)\n\t"
      "v_mul_f32_e32 v40, v233, v40\n\t"
      "v_mul_f32_e32 v41, v233, v41\n\t"
      "v_mul_f32_e32 v42, v233, v42\n\t"
      "v_mfma_f32_16x16x32_bf16 v[44:47], a[108:111], v[208:211], v[44:47]\n\t"
      "v_mul_f32_e32 v43, v233, v43\n\t"
      "v_mfma_f32_16x16x32_bf16 v[64:67], a[0:3], v[176:179], v[64:67]\n\t"
      "v_mfma_f32_16x16x32_bf16 v[64:67], a[4:7], v[188:191], v[64:67]\n\t"
      "s_waitcnt lgkmcnt(2)\n\t"
      "buffer_store_dwordx4 v[80:83], v14, s[88:91], s45 offen offset:0 sc1\n\t"
      "v_mfma_f32_16x16x32_bf16 v[64:67], a[80:83], v[200:203], v[64:67]\n\t"
      "s_waitcnt lgkmcnt(1)\n\t"
      "buffer_store_dwordx4 v[84:87], v14, s[88:91], s45 offen offset:2048 sc1\n\t"
      "v_mul_f32_e32 v44, v233, v44\n\t"
      "v_mul_f32_e32 v45, v233, v45\n\t"
      "v_mfma_f32_16x16x32_bf16 v[64:67], a[84:87], v[212:215], v[64:67]\n\t"
      "v_mul_f32_e32 v46, v233, v46\n\t"
      "v_mul_f32_e32 v47, v233, v47\n\t"
      "v_mfma_f32_16x16x32_bf16 v[68:71], a[8:11], v[176:179], v[68:71]\n\t"
      "v_cvt_pk_bf16_f32 v152, v40, v41\n\t"
      "v_cvt_pk_bf16_f32 v153, v42, v43\n\t"
      "v_mfma_f32_16x16x32_bf16 v[68:71], a[12:15], v[188:191], v[68:71]\n\t"
      "v_cvt_pk_bf16_f32 v154, v44, v45\n\t"
      "v_cvt_pk_bf16_f32 v155, v46, v47\n\t"
      "v_mfma_f32_16x16x32_bf16 v[68:71], a[88:91], v[200:203], v[68:71]\n\t"
      "ds_write_b128 v240, v[152:155] offset:22528\n\t"
      "v_mul_f32_e32 v64, v234, v64\n\t"
      "v_mul_f32_e32 v65, v234, v65\n\t"
      "v_mfma_f32_16x16x32_bf16 v[68:71], a[92:95], v[212:215], v[68:71]\n\t"
      "ds_read_b128 v[32:35], v13 offset:22528\n\t"
      "v_mul_f32_e32 v66, v234, v66\n\t"
      "v_mul_f32_e32 v67, v234, v67\n\t"
      "v_mfma_f32_16x16x32_bf16 v[72:75], a[16:19], v[176:179], v[72:75]\n\t"
      "ds_read_b128 v[36:39], v13 offset:23552\n\t"
      "v_mfma_f32_16x16x32_bf16 v[72:75], a[20:23], v[188:191], v[72:75]\n\t"
      "v_mfma_f32_16x16x32_bf16 v[72:75], a[96:99], v[200:203], v[72:75]\n\t"
      "s_nop 0\n\t"
      "v_mul_f32_e32 v68, v234, v68\n\t"
      "v_mul_f32_e32 v69, v234, v69\n\t"
      "v_mfma_f32_16x16x32_bf16 v[72:75], a[100:103], v[212:215], v[72:75]\n\t"
      "v_mul_f32_e32 v70, v234, v70\n\t"
      "v_mul_f32_e32 v71, v234, v71\n\t"
      "v_mfma_f32_16x16x32_bf16 v[76:79], a[24:27], v[176:179], v[76:79]\n\t"
      "v_cvt_pk_bf16_f32 v156, v64, v65\n\t"
      "v_cvt_pk_bf16_f32 v157, v66, v67\n\t"
      "v_mfma_f32_16x16x32_bf16 v[76:79], a[28:31], v[188:191], v[76:79]\n\t"
      "s_waitcnt lgkmcnt(1)\n\t"
      "buffer_store_dwordx4 v[32:35], v14, s[88:91], s36 offen offset:128 sc1\n\t"
      "v_cvt_pk_bf16_f32 v158, v68, v69\n\t"
      "v_cvt_pk_bf16_f32 v159, v70, v71\n\t"
      "v_mfma_f32_16x16x32_bf16 v[76:79], a[104:107], v[200:203], v[76:79]\n\t"
      "s_waitcnt lgkmcnt(0)\n\t"
      "buffer_store_dwordx4 v[36:39], v14, s[88:91], s36 offen offset:2176 sc1\n\t"
      "v_mul_f32_e32 v72, v234, v72\n\t"
      "v_mul_f32_e32 v73, v234, v73\n\t"
      "v_mfma_f32_16x16x32_bf16 v[76:79], a[108:111], v[212:215], v[76:79]\n\t"
      "ds_write_b128 v12, v[156:159] offset:24576\n\t"
      "v_mul_f32_e32 v74, v234, v74\n\t"
      "v_mul_f32_e32 v75, v234, v75\n\t"
      "v_mfma_f32_16x16x32_bf16 v[96:99], a[0:3], v[180:183], v[96:99]\n\t"
      "v_mfma_f32_16x16x32_bf16 v[96:99], a[4:7], v[192:195], v[96:99]\n\t"
      "v_mfma_f32_16x16x32_bf16 v[96:99], a[80:83], v[204:207], v[96:99]\n\t"
      "s_nop 1\n\t"
      "v_mul_f32_e32 v76, v234, v76\n\t"
      "v_mul_f32_e32 v77, v234, v77\n\t"
      "v_mfma_f32_16x16x32_bf16 v[96:99], a[84:87], v[216:219], v[96:99]\n\t"
      "v_mul_f32_e32 v78, v234, v78\n\t"
      "v_mul_f32_e32 v79, v234, v79\n\t"
      "v_mfma_f32_16x16x32_bf16 v[100:103], a[8:11], v[180:183], v[100:103]\n\t"
      "v_cvt_pk_bf16_f32 v160, v72, v73\n\t"
      "v_cvt_pk_bf16_f32 v161, v74, v75\n\t"
      "v_mfma_f32_16x16x32_bf16 v[100:103], a[12:15], v[192:195], v[100:103]\n\t"
      "v_cvt_pk_bf16_f32 v162, v76, v77\n\t"
      "v_cvt_pk_bf16_f32 v163, v78, v79\n\t"
      "v_mfma_f32_16x16x32_bf16 v[100:103], a[88:91], v[204:207], v[100:103]\n\t"
      "ds_write_b128 v240, v[160:163] offset:24576\n\t"
      "v_mul_f32_e32 v96, v235, v96\n\t"
      "v_mul_f32_e32 v97, v235, v97\n\t"
      "v_mfma_f32_16x16x32_bf16 v[100:103], a[92:95], v[216:219], v[100:103]\n\t"
      "ds_read_b128 v[64:67], v13 offset:24576\n\t"
      "v_mul_f32_e32 v98, v235, v98\n\t"
      "v_mul_f32_e32 v99, v235, v99\n\t"
      "v_mfma_f32_16x16x32_bf16 v[104:107], a[16:19], v[180:183], v[104:107]\n\t"
      "ds_read_b128 v[68:71], v13 offset:25600\n\t"
      "v_mfma_f32_16x16x32_bf16 v[104:107], a[20:23], v[192:195], v[104:107]\n\t"
      "v_mfma_f32_16x16x32_bf16 v[104:107], a[96:99], v[204:207], v[104:107]\n\t"
      "s_nop 0\n\t"
      "v_mul_f32_e32 v100, v235, v100\n\t"
      "v_mul_f32_e32 v101, v235, v101\n\t"
      "v_mfma_f32_16x16x32_bf16 v[104:107], a[100:103], v[216:219], v[104:107]\n\t"
      "v_mul_f32_e32 v102, v235, v102\n\t"
      "v_mul_f32_e32 v103, v235, v103\n\t"
      "v_mfma_f32_16x16x32_bf16 v[108:111], a[24:27], v[180:183], v[108:111]\n\t"
      "v_cvt_pk_bf16_f32 v164, v96, v97\n\t"
      "v_cvt_pk_bf16_f32 v165, v98, v99\n\t"
      "v_mfma_f32_16x16x32_bf16 v[108:111], a[28:31], v[192:195], v[108:111]\n\t"
      "s_waitcnt lgkmcnt(1)\n\t"
      "buffer_store_dwordx4 v[64:67], v14, s[88:91], s44 offen offset:128 sc1\n\t"
      "v_cvt_pk_bf16_f32 v166, v100, v101\n\t"
      "v_cvt_pk_bf16_f32 v167, v102, v103\n\t"
      "v_mfma_f32_16x16x32_bf16 v[108:111], a[104:107], v[204:207], v[108:111]\n\t"
      "s_waitcnt lgkmcnt(0)\n\t"
      "buffer_store_dwordx4 v[68:71], v14, s[88:91], s44 offen offset:2176 sc1\n\t"
      "v_mul_f32_e32 v104, v235, v104\n\t"
      "v_mul_f32_e32 v105, v235, v105\n\t"
      "v_mfma_f32_16x16x32_bf16 v[108:111], a[108:111], v[216:219], v[108:111]\n\t"
      "ds_write_b128 v12, v[164:167] offset:26624\n\t"
      "v_mul_f32_e32 v106, v235, v106\n\t"
      "v_mul_f32_e32 v107, v235, v107\n\t"
      "s_nop 4\n\t"
      "v_mul_f32_e32 v108, v235, v108\n\t"
      "v_mul_f32_e32 v109, v235, v109\n\t"
      "v_mul_f32_e32 v110, v235, v110\n\t"
      "v_mul_f32_e32 v111, v235, v111\n\t"
      "v_cvt_pk_bf16_f32 v168, v104, v105\n\t"
      "v_cvt_pk_bf16_f32 v169, v106, v107\n\t"
      "v_cvt_pk_bf16_f32 v170, v108, v109\n\t"
      "v_cvt_pk_bf16_f32 v171, v110, v111\n\t"
      "ds_write_b128 v240, v[168:171] offset:26624\n\t"
      "ds_read_b128 v[96:99], v13 offset:26624\n\t"
      "ds_read_b128 v[100:103], v13 offset:27648\n\t"
      "s_waitcnt lgkmcnt(1)\n\t"
      "buffer_store_dwordx4 v[96:99], v14, s[88:91], s45 offen offset:128 sc1\n\t"
      "s_waitcnt lgkmcnt(0)\n\t"
      "buffer_store_dwordx4 v[100:103], v14, s[88:91], s45 offen offset:2176 sc1\n\t"
      "s_cbranch_vccz .Lok_%=\n\t"
      "ds_write_b32 v238, v232\n\t"
      ".Lok_%=:\n\t"
      "s_waitcnt lgkmcnt(0)\n\t"
      "s_barrier\n\t"
      "ds_read_b32 v232, v238\n\t"
      "s_waitcnt lgkmcnt(0)\n\t"
      "v_readfirstlane_b32 %[flag], v232\n\t"
      "s_cmp_lg_u32 %[flag], 0\n\t"
      "s_cbranch_scc1 .Lc1_%=\n\t"
      "s_endpgm\n\t"
      ".Lc1_%=:\n\t"
      : [flag] "=&s"(flag), [mc0] "+v"(mc0), [mc1] "+v"(mc1), [mc2] "+v"(mc2)
      : [ka] "s"(ka), [bid] "s"(bid), [pre] "n"(PRE), [tid] "v"(tid)
      : "v8", "v9", "v10", "v11", "v12", "v13", "v14", "v15", "v16", "v17", "v18", "v19", "v20", "v21", "v22", "v23", "v24", "v25", "v26", "v27", "v28", "v29", "v30", "v31",
        "v32", "v33", "v34", "v35", "v36", "v37", "v38", "v39", "v40", "v41", "v42", "v43", "v44", "v45", "v46", "v47", "v48", "v49", "v50", "v51", "v52", "v53", "v54", "v55",
        "v56", "v57", "v58", "v59", "v60", "v61", "v62", "v63", "v64", "v65", "v66", "v67", "v68", "v69", "v70", "v71", "v72", "v73", "v74", "v75", "v76", "v77", "v78", "v79",
        "v80", "v81", "v82", "v83", "v84", "v85", "v86", "v87", "v88", "v89", "v90", "v91", "v92", "v93", "v94", "v95", "v96", "v97", "v98", "v99", "v100", "v101", "v102", "v103",
        "v104", "v105", "v106", "v107", "v108", "v109", "v110", "v111", "v112", "v113", "v114", "v115", "v116", "v117", "v118", "v119", "v120", "v121", "v122", "v123", "v124", "v125", "v126", "v127",
        "v128", "v129", "v130", "v131", "v132", "v133", "v134", "v135", "v136", "v137", "v138", "v139", "v140", "v141", "v142", "v143", "v144", "v145", "v146", "v147", "v148", "v149", "v150", "v151",
        "v152", "v153", "v154", "v155", "v156", "v157", "v158", "v159", "v160", "v161", "v162", "v163", "v164", "v165", "v166", "v167", "v168", "v169", "v170", "v171", "v172", "v173", "v174", "v175",
        "v176", "v177", "v178", "v179", "v180", "v181", "v182", "v183", "v184", "v185", "v186", "v187", "v188", "v189", "v190", "v191", "v192", "v193", "v194", "v195", "v196", "v197", "v198", "v199",
        "v200", "v201", "v202", "v203", "v204", "v205", "v206", "v207", "v208", "v209", "v210", "v211", "v212", "v213", "v214", "v215", "v216", "v217", "v218", "v219", "v220", "v221", "v222", "v223",
        "v224", "v225", "v226", "v227", "v228", "v229", "v230", "v231", "v232", "v233", "v234", "v235", "v236", "v237", "v238", "v239", "v240", "v241", "v242", "v243", "v244", "v245", "v246", "v247",
        "a0", "a1", "a2", "a3", "a4", "a5", "a6", "a7", "a8", "a9", "a10", "a11", "a12", "a13", "a14", "a15", "a16", "a17", "a18", "a19", "a20", "a21", "a22", "a23",
        "a24", "a25", "a26", "a27", "a28", "a29", "a30", "a31", "a32", "a33", "a34", "a35", "a36", "a37", "a38", "a39", "a40", "a41", "a42", "a43", "a44", "a45", "a46", "a47",
        "a48", "a49", "a50", "a51", "a52", "a53", "a54", "a55", "a56", "a57", "a58", "a59", "a60", "a61", "a62", "a63", "a64", "a65", "a66", "a67", "a68", "a69", "a70", "a71",
        "a72", "a73", "a74", "a75", "a76", "a77", "a78", "a79", "a80", "a81", "a82", "a83", "a84", "a85", "a86", "a87", "a88", "a89", "a90", "a91", "a92", "a93", "a94", "a95",
        "a96", "a97", "a98", "a99", "a100", "a101", "a102", "a103", "a104", "a105", "a106", "a107", "a108", "a109", "a110", "a111", "a112", "a113", "a114", "a115", "a116", "a117", "a118", "a119",
        "a120", "a121", "a122", "a123", "a124", "a125", "a126", "a127", "s20", "s21", "s22", "s23", "s24", "s25", "s26", "s27", "s28", "s29", "s30", "s31", "s34", "s35", "s36", "s37",
        "s38", "s39", "s40", "s41", "s42", "s43", "s44", "s45", "s46", "s47", "s48", "s49", "s50", "s51", "s52", "s53", "s54", "s55", "s56", "s57", "s58", "s59", "s82", "s83",
        "s84", "s85", "s86", "s87", "s88", "s89", "s90", "s91", "s92", "s93", "s94", "s95", "s96", "s97", "s98", "s99", "vcc", "scc", "memory");
}

__global__ __launch_bounds__(256, 1) void attn_fwd(const uint16_t* __restrict__ Q, const uint16_t* __restrict__ K,
                                                   const uint16_t* __restrict__ V, uint16_t* __restrict__ O) {
  unsigned tid = threadIdx.x, bid = blockIdx.x;
  const void* ka = (const void*)__builtin_amdgcn_kernarg_segment_ptr();
  float mc0, mc1, mc2;
  asm volatile("" : "=v"(mc0), "=v"(mc1), "=v"(mc2));
  attn_pass<0>(ka, bid, tid, mc0, mc1, mc2);
  asm volatile("" : "+v"(tid), "+s"(bid), "+s"(ka));
  typedef const uint16_t* cptr;
  const __attribute__((address_space(4))) cptr* kp = (const __attribute__((address_space(4))) cptr*)(unsigned long long)ka;
  const unsigned b2 = bid, head = (b2 & 7u) * 32u + (b2 >> 3);
  const uint16_t* qr = kp[0] + ((size_t)head * SQ + 48u * (tid >> 6) + (tid & 15u)) * HD;
  const uint16_t* Kh = kp[1] + (size_t)head * SK * HD;
  mc0 = true_row_max(qr, Kh, (tid >> 4) & 3u);
  mc1 = true_row_max(qr + 16 * HD, Kh, (tid >> 4) & 3u);
  mc2 = true_row_max(qr + 32 * HD, Kh, (tid >> 4) & 3u);
  __syncthreads();
  attn_pass<1>(ka, bid, tid, mc0, mc1, mc2);
}

extern "C" void kernel_launch(void* const* d_in, const int* in_sizes, int n_in, void* d_out, int out_size, void* d_ws, size_t ws_size,
                              hipStream_t stream) {
  (void)in_sizes; (void)n_in; (void)out_size; (void)d_ws; (void)ws_size;
  (void)hipFuncSetAttribute((const void*)attn_fwd, hipFuncAttributeMaxDynamicSharedMemorySize, (int)LDS_BYTES);
  hipLaunchKernelGGL(attn_fwd, dim3(NHEAD), dim3(256), LDS_BYTES, stream, (const uint16_t*)d_in[0], (const uint16_t*)d_in[1],
                     (const uint16_t*)d_in[2], (uint16_t*)d_out);
}
```
